```python
import torch

def _softmax(x, axis=-1):
    return torch.softmax(x, dim=axis)
def _gelu(x):
    return torch.nn.functional.gelu(x, approximate="tanh")
def _silu(x):
    return torch.nn.functional.silu(x)
def _sigmoid(x):
    return torch.sigmoid(x)
def _layernorm(x, axis=-1, eps=1e-5):
    m = x.mean(dim=axis, keepdim=True)
    v = x.var(dim=axis, keepdim=True, unbiased=False)
    return (x - m) / torch.sqrt(v + eps)
def _scatter_add(tgt, idx, src, axis=0):
    return tgt.clone().index_add_(axis, idx, src)
def _segment_sum(src, seg, n):
    out = torch.zeros((n,) + src.shape[1:], dtype=src.dtype)
    return out.index_add_(0, seg, src)
def _pad(x, pw):
    flat = tuple(v for pair in reversed(pw) for v in pair)
    return torch.nn.functional.pad(x, flat)

kernel_name = 'standalone__window_attn_swin_v2__vision'

def setup_inputs():

    _w = (lambda a: (lambda t: t.to(torch.bfloat16) if t.is_floating_point() else t)(torch.as_tensor(a)))
    win = 7
    H = W = (384 // win) * win
    C = 96
    nh = 4
    shift = win // 2
    coords = torch.stack(torch.meshgrid(torch.arange(win), torch.arange(win), indexing="ij"), -1).reshape(-1, 2)
    rel = coords[:, None, :] - coords[None, :, :]
    rel_idx = ((rel[..., 0] + win - 1) * (2 * win - 1) + (rel[..., 1] + win - 1)).to(torch.int64)
    tab = torch.stack(torch.meshgrid(torch.arange(1 - win, win), torch.arange(1 - win, win), indexing="ij"), -1).reshape(-1, 2).to(float)
    tab = (lambda _x: torch.sign(torch.as_tensor(_x)))(tab) * (lambda _x: torch.log1p(torch.as_tensor(_x)))(torch.abs(tab))
    img = torch.zeros((H, W), dtype=torch.int64)
    cnt = 0
    for hs in (slice(0, H - win), slice(H - win, H - shift), slice(H - shift, H)):
        for ws in (slice(0, W - win), slice(W - win, W - shift), slice(W - shift, W)):
            img[hs, ws] = cnt; cnt += 1
    mw = img.reshape(H // win, win, W // win, win).permute(0, 2, 1, 3).reshape(-1, win * win)
    mask = (mw[:, :, None] != mw[:, None, :]).to(float) * (-100.0)
    return {
        "x": _w((lambda *s: torch.randn(*s, dtype=torch.bfloat16) * 0.05)(1, H, W, C)),
        "wqkv": _w((lambda *s: torch.randn(*s, dtype=torch.bfloat16) * 0.8)(C, 3 * C)),
        "wo": _w((lambda *s: torch.randn(*s, dtype=torch.bfloat16) * 0.05)(C, C)),
        "logit_scale": _w((lambda *s: torch.randn(*s, dtype=torch.bfloat16) * 0.8)(nh)),
        "cpb_w1": _w((lambda *s: torch.randn(*s, dtype=torch.bfloat16) * 0.8)(2, 4 * nh)),
        "cpb_w2": _w((lambda *s: torch.randn(*s, dtype=torch.bfloat16) * 0.8)(4 * nh, nh)),
        "rel_coords": _w(tab),
        "rel_idx": _w(rel_idx),
        "attn_mask": _w(mask),
    }

def reference(x, wqkv, wo, logit_scale, cpb_w1, cpb_w2, rel_coords, rel_idx, attn_mask):
    B, H, W, C = x.shape
    win = 7
    nh = 4
    hd = C // nh
    shift = win // 2
    ww = win * win
    nH, nW = H // win, W // win
    xs = torch.roll(x, (-shift, -shift), (1, 2))
    xw = (lambda x, *d: x.permute(*d))(xs.reshape(B, nH, win, nW, win, C), 0, 1, 3, 2, 4, 5).reshape(B * nH * nW, ww, C)
    qkv = (lambda x, *d: x.permute(*d))((xw @ wqkv).reshape(B * nH * nW, ww, 3, nh, hd), 2, 0, 3, 1, 4)
    q, k, v = qkv[0], qkv[1], qkv[2]
    qn = q * (torch.einsum("bhqd,bhqd->bhq", q, q)[..., None] + 1e-12) ** -0.5
    kn = k * (torch.einsum("bhkd,bhkd->bhk", k, k)[..., None] + 1e-12) ** -0.5
    logits = torch.einsum("bhqd,bhkd->bhqk", qn, kn)
    tau = torch.exp(torch.clip(logit_scale, -100.0, 4.6052))
    logits = logits * tau[None, :, None, None]
    hcpb = rel_coords @ cpb_w1
    hcpb = torch.where(hcpb > 0, hcpb, hcpb * 0)
    bias_tab = 16.0 * _sigmoid(hcpb @ cpb_w2)
    bias = (lambda x, *d: x.permute(*d))(bias_tab[rel_idx], 2, 0, 1)
    logits = logits.reshape(B, nH * nW, nh, ww, ww) + bias[None, None, :, :, :] + attn_mask[None, :, None, :, :]
    p = _softmax(logits.reshape(B * nH * nW, nh, ww, ww), axis=-1)
    o = (lambda x, *d: x.permute(*d))(torch.einsum("bhqk,bhkd->bhqd", p, v), 0, 2, 1, 3).reshape(B * nH * nW, ww, C) @ wo
    o = (lambda x, *d: x.permute(*d))(o.reshape(B, nH, nW, win, win, C), 0, 1, 3, 2, 4, 5).reshape(B, H, W, C)
    o = torch.roll(o, (shift, shift), (1, 2))
    return _layernorm(o, axis=-1)
```

```cpp
#include <hip/hip_runtime.h>
#include <hip/hip_bf16.h>
#include <stdint.h>

#if defined(__HIP_DEVICE_COMPILE__)
#define TGT __attribute__((target("no-packed-fp32-ops")))
#else
#define TGT
#endif
#define DI __device__ __forceinline__ TGT

typedef __attribute__((ext_vector_type(16))) float f32x16;
typedef __attribute__((ext_vector_type(4))) float f32x4;
typedef __attribute__((ext_vector_type(2))) float f32x2;
typedef __attribute__((ext_vector_type(8))) __bf16 bf16x8;
typedef __attribute__((ext_vector_type(2))) __bf16 bf16x2;
typedef __attribute__((ext_vector_type(8))) _Float16 f16x8;
typedef __attribute__((ext_vector_type(4))) _Float16 f16x4;
typedef __attribute__((ext_vector_type(2))) _Float16 f16x2;
typedef __attribute__((ext_vector_type(4))) short s16x4;
typedef __attribute__((ext_vector_type(8))) short s16x8;
typedef __attribute__((ext_vector_type(4))) unsigned u32x4;
typedef __attribute__((ext_vector_type(2))) unsigned u32x2;
typedef __attribute__((ext_vector_type(4))) int i32x4;
typedef u32x4 u32x4_a2 __attribute__((aligned(2)));
typedef u32x2 u32x2_a2 __attribute__((aligned(2)));
typedef i32x4 i32x4_a4 __attribute__((aligned(4)));
typedef __attribute__((address_space(3))) char* lds_ptr;
#define LDS_AS __attribute__((address_space(3)))

constexpr int IMG = 378;
constexpr int NWIN = 54;
constexpr unsigned PIXB = 192;
constexpr unsigned ROWB = IMG * PIXB;
constexpr unsigned MASKB = 49 * 49 * 2;
constexpr float LOG2E = 1.4426950408889634f;

constexpr int W_OFF = 0;
constexpr int WO_OFF = 61440;
constexpr int BI_OFF = 79872;
constexpr int BS_OFF = 129024;
constexpr int EX_OFF = 131072;
constexpr int FL_OFF = 155648;
constexpr int TB_OFF = 155776;
constexpr int LDS_BYTES = 158720;

DI float bf2f(unsigned short u) { return __builtin_bit_cast(float, (unsigned)u << 16); }
DI float bflo(unsigned u) { return __builtin_bit_cast(float, u << 16); }
DI float bfhi(unsigned u) { return __builtin_bit_cast(float, u & 0xffff0000u); }
DI float bfr(float x) { return (float)(__bf16)x; }
DI unsigned pkbf(float a, float b) { f32x2 v = {a, b}; return __builtin_bit_cast(unsigned, __builtin_convertvector(v, bf16x2)); }
DI f16x2 pkh(float a, float b) { f32x2 v = {a, b}; return __builtin_convertvector(v, f16x2); }
DI unsigned asu(float x) { return __builtin_bit_cast(unsigned, x); }
DI float asf(unsigned x) { return __builtin_bit_cast(float, x); }

DI bf16x8 trfrag(lds_ptr p, int off0, int off1) {
    s16x4 lo = __builtin_amdgcn_ds_read_tr16_b64_v4i16((LDS_AS s16x4*)(p + off0));
    s16x4 hi = __builtin_amdgcn_ds_read_tr16_b64_v4i16((LDS_AS s16x4*)(p + off1));
    return __builtin_bit_cast(bf16x8, __builtin_shufflevector(lo, hi, 0, 1, 2, 3, 4, 5, 6, 7));
}
#define MFMA_BF(a, b, c) __builtin_amdgcn_mfma_f32_32x32x16_bf16((a), (b), (c), 0, 0, 0)
#define MFMA_BF8(a, b, c) __builtin_amdgcn_mfma_f32_32x32x8bf16_1k((a), (b), (c), 0, 0, 0)
#define MFMA_H(a, b, c) __builtin_amdgcn_mfma_f32_32x32x16_f16((a), (b), (c), 0, 0, 0)
#define MFMA_H8(a, b, c) __builtin_amdgcn_mfma_f32_32x32x8f16((a), (b), (c), 0, 0, 0)

struct HeadVec { f16x8 lo; f16x4 hi; };

DI HeadVec mkhv(const f16x2* h) {
    HeadVec r;
    u32x4 a = {__builtin_bit_cast(unsigned, h[0]), __builtin_bit_cast(unsigned, h[1]),
               __builtin_bit_cast(unsigned, h[2]), __builtin_bit_cast(unsigned, h[3])};
    u32x2 b = {__builtin_bit_cast(unsigned, h[4]), __builtin_bit_cast(unsigned, h[5])};
    r.lo = __builtin_bit_cast(f16x8, a);
    r.hi = __builtin_bit_cast(f16x4, b);
    return r;
}

template <bool SLOW, bool SCALE>
DI void norm_pair(const float* va, const float* vb, float c, HeadVec& oa, HeadVec& ob, float& mn, float& mx) {
    f16x2 ha[6], hb[6];
    if constexpr (!SLOW) {
#pragma unroll
        for (int i = 0; i < 6; ++i) { ha[i] = pkh(va[2 * i], va[2 * i + 1]); hb[i] = pkh(vb[2 * i], vb[2 * i + 1]); }
        f16x2 sa = ha[0] * ha[0], sb = hb[0] * hb[0];
#pragma unroll
        for (int i = 1; i < 6; ++i) { sa = __builtin_elementwise_fma(ha[i], ha[i], sa); sb = __builtin_elementwise_fma(hb[i], hb[i], sb); }
        auto r = __builtin_amdgcn_permlane32_swap(__builtin_bit_cast(unsigned, sa), __builtin_bit_cast(unsigned, sb), false, false);
        f16x2 t = __builtin_bit_cast(f16x2, (unsigned)r[0]) + __builtin_bit_cast(f16x2, (unsigned)r[1]);
        float ss = __builtin_fmaf((float)t[0], 1.0f, (float)t[1]);
        mn = __builtin_fminf(mn, ss);
        mx = __builtin_fmaxf(mx, ss);
        float s = __builtin_amdgcn_rsqf(ss);
        if constexpr (SCALE) s *= c;
        unsigned s2 = __builtin_bit_cast(unsigned, pkh(s, s));
        auto q = __builtin_amdgcn_permlane32_swap(s2, s2, false, false);
        f16x2 fa = __builtin_bit_cast(f16x2, (unsigned)q[0]), fb = __builtin_bit_cast(f16x2, (unsigned)q[1]);
#pragma unroll
        for (int i = 0; i < 6; ++i) { ha[i] *= fa; hb[i] *= fb; }
    } else {
        float sa = va[0] * va[0], sb = vb[0] * vb[0];
#pragma unroll
        for (int i = 1; i < 12; ++i) { sa = __builtin_fmaf(va[i], va[i], sa); sb = __builtin_fmaf(vb[i], vb[i], sb); }
        auto r = __builtin_amdgcn_permlane32_swap(asu(sa), asu(sb), false, false);
        float ss = asf(r[0]) + asf(r[1]) + 1e-12f;
        float s = __builtin_amdgcn_rsqf(ss);
        if constexpr (SCALE) s *= c;
        auto q = __builtin_amdgcn_permlane32_swap(asu(s), asu(s), false, false);
        float fa = asf(q[0]), fb = asf(q[1]);
#pragma unroll
        for (int i = 0; i < 6; ++i) { ha[i] = pkh(va[2 * i] * fa, va[2 * i + 1] * fa); hb[i] = pkh(vb[2 * i] * fb, vb[2 * i + 1] * fb); }
    }
    oa = mkhv(ha);
    ob = mkhv(hb);
}

struct QKV {
    HeadVec Ka[2], Kb[2], Qa[2], Qb[2];
    u32x4 V[2][3];
    u32x2 V12;
};

template <bool SLOW>
DI void qk_phase(const bf16x8* xa, const bf16x8* xb, lds_ptr wp, float c0, float c1, QKV& o, float& mn, float& mx) {
    float fa[48], fb[48];
#pragma unroll
    for (int m = 0; m < 3; ++m) {
        f32x16 aa = {}, ab = {};
#pragma unroll
        for (int s = 0; s < 6; ++s) {
            bf16x8 w = trfrag(wp, m * 6144 + s * 1024, m * 6144 + s * 1024 + 256);
            aa = MFMA_BF(w, xa[s], aa);
            ab = MFMA_BF(w, xb[s], ab);
        }
#pragma unroll
        for (int r = 0; r < 16; ++r) { fa[16 * m + r] = aa[r]; fb[16 * m + r] = ab[r]; }
    }
    norm_pair<SLOW, false>(fa, fb, 1.f, o.Ka[0], o.Kb[0], mn, mx);
    norm_pair<SLOW, false>(fa + 12, fb + 12, 1.f, o.Ka[1], o.Kb[1], mn, mx);
    norm_pair<SLOW, true>(fa + 24, fb + 24, c0, o.Qa[0], o.Qb[0], mn, mx);
    norm_pair<SLOW, true>(fa + 36, fb + 36, c1, o.Qa[1], o.Qb[1], mn, mx);
}

DI void v_phase(const bf16x8* xa, const bf16x8* xb, lds_ptr wp, lds_ptr onep, QKV& o) {
    f32x16 ones;
#pragma unroll
    for (int i = 0; i < 4; ++i) {
        f32x4 u = *(volatile LDS_AS f32x4*)(onep);
#pragma unroll
        for (int r = 0; r < 4; ++r) ones[4 * i + r] = u[r];
    }
#pragma unroll
    for (int hd = 0; hd < 2; ++hd) {
        f32x16 va = ones, vb = ones;
#pragma unroll
        for (int s = 0; s < 6; ++s) {
            bf16x8 w = trfrag(wp, (3 + hd) * 6144 + s * 1024, (3 + hd) * 6144 + s * 1024 + 256);
            va = MFMA_BF(xa[s], w, va);
            vb = MFMA_BF(xb[s], w, vb);
        }
        o.V[hd][0] = u32x4{pkbf(va[0], va[1]), pkbf(va[2], va[3]), pkbf(va[4], va[5]), pkbf(va[6], va[7])};
        o.V[hd][1] = u32x4{pkbf(va[8], va[9]), pkbf(va[10], va[11]), pkbf(vb[0], vb[1]), pkbf(vb[2], vb[3])};
        o.V[hd][2] = u32x4{pkbf(vb[4], vb[5]), pkbf(vb[6], vb[7]), pkbf(vb[8], vb[9]), pkbf(vb[10], vb[11])};
        o.V12[hd] = pkbf(va[12], va[13]);
    }
}

template <bool SLOW>
DI void attn_one(const QKV& t, const HeadVec& Q, int hd, lds_ptr bp, int boff, lds_ptr bsp, int bsoff,
                 const unsigned* mk, unsigned* od) {
    f32x16 sa, sb;
#pragma unroll
    for (int i = 0; i < 3; ++i) {
        f32x4 u = *(LDS_AS f32x4*)(bp + boff + i * 1024);
        f32x4 v = *(LDS_AS f32x4*)(bp + boff + (3 + i) * 1024);
#pragma unroll
        for (int r = 0; r < 4; ++r) { sa[4 * i + r] = u[r]; sb[4 * i + r] = v[r]; }
    }
    sa[12] = *(LDS_AS float*)(bsp + bsoff);
    sa = MFMA_H(t.Ka[hd].lo, Q.lo, sa);
    sb = MFMA_H(t.Kb[hd].lo, Q.lo, sb);
    sa = MFMA_H8(t.Ka[hd].hi, Q.hi, sa);
    sb = MFMA_H8(t.Kb[hd].hi, Q.hi, sb);
    float p[25];
#pragma unroll
    for (int r = 0; r < 12; ++r) { p[r] = sa[r]; p[12 + r] = sb[r]; }
    p[24] = sa[12];
    if constexpr (SLOW) {
#pragma unroll
        for (int i = 0; i < 12; ++i) {
            p[2 * i] = __builtin_fmaf(bflo(mk[i]), LOG2E, p[2 * i]);
            p[2 * i + 1] = __builtin_fmaf(bfhi(mk[i]), LOG2E, p[2 * i + 1]);
        }
        p[24] = __builtin_fmaf(bflo(mk[12]), LOG2E, p[24]);
        float m = p[24];
#pragma unroll
        for (int i = 0; i < 12; ++i) m = __builtin_fmaxf(__builtin_fmaxf(m, p[2 * i]), p[2 * i + 1]);
        auto r = __builtin_amdgcn_permlane32_swap(asu(m), asu(m), false, false);
        m = __builtin_fmaxf(asf(r[0]), asf(r[1]));
#pragma unroll
        for (int i = 0; i < 25; ++i) p[i] -= m;
    }
#pragma unroll
    for (int i = 0; i < 25; ++i) p[i] = __builtin_amdgcn_exp2f(p[i]);
    f32x16 o = {};
#pragma unroll
    for (int s = 0; s < 3; ++s) {
        u32x4 pk = {pkbf(p[8 * s], p[8 * s + 1]), pkbf(p[8 * s + 2], p[8 * s + 3]), pkbf(p[8 * s + 4], p[8 * s + 5]), pkbf(p[8 * s + 6], p[8 * s + 7])};
        o = MFMA_BF(__builtin_bit_cast(bf16x8, t.V[hd][s]), __builtin_bit_cast(bf16x8, pk), o);
    }
    unsigned pl = pkbf(p[24], 0.f);
    u32x2 p2 = hd == 0 ? u32x2{pl, 0u} : u32x2{0u, pl};
    o = MFMA_BF8(__builtin_bit_cast(s16x4, t.V12), __builtin_bit_cast(s16x4, p2), o);
    float rl = __builtin_amdgcn_rcpf(o[12]);
#pragma unroll
    for (int i = 0; i < 6; ++i) od[i] = pkbf(o[2 * i] * rl, o[2 * i + 1] * rl);
}

DI void load_mask_rows(const char* mwin, int tok, int own, int oth, int hh, unsigned* mk) {
    const unsigned qoff = tok * 98;
#pragma unroll
    for (int i = 0; i < 3; ++i) {
        u32x2 a = *(const u32x2_a2*)(mwin + (qoff + (own + 8 * i + 4 * hh) * 2));
        u32x2 b = *(const u32x2_a2*)(mwin + (qoff + (oth + 8 * i + 4 * hh) * 2));
        mk[2 * i] = a[0]; mk[2 * i + 1] = a[1];
        mk[6 + 2 * i] = b[0]; mk[6 + 2 * i + 1] = b[1];
    }
    mk[12] = *(const unsigned short*)(mwin + (qoff + 48));
}

DI void win_of(int pair, int u, int& wy, int& wx) {
    int idx = pair < 868 ? u * 868 + pair : 2604 + 156 * u + (pair - 868);
    int j = idx - 2809;
    int q = (idx * 19785) >> 20;
    wy = j < 0 ? q : (j < 54 ? 53 : j - 54);
    wx = j < 0 ? idx - q * 53 : (j < 54 ? j : 53);
}

DI int lane_id() { return __builtin_amdgcn_mbcnt_hi(~0u, __builtin_amdgcn_mbcnt_lo(~0u, 0u)); }
DI void wg_barrier() {
    __builtin_amdgcn_fence(__ATOMIC_RELEASE, "workgroup", "local");
    __builtin_amdgcn_s_barrier();
    __builtin_amdgcn_fence(__ATOMIC_ACQUIRE, "workgroup", "local");
}
DI float uni(float x) { return asf(__builtin_amdgcn_readfirstlane(asu(x))); }

DI void tok_offs(int wy, int wx, unsigned la, unsigned lb, unsigned& oa, unsigned& ob) {
    const unsigned base = (unsigned)(7 * wy + 3) * ROWB + (unsigned)(7 * wx + 3) * PIXB;
    oa = base + la;
    ob = base + lb;
    if (wy == NWIN - 1) {
        oa -= la >= 4 * ROWB ? IMG * ROWB : 0;
        ob -= lb >= 4 * ROWB ? IMG * ROWB : 0;
    }
    if (wx == NWIN - 1) {
        oa -= la % ROWB >= 4 * PIXB ? ROWB : 0;
        ob -= lb % ROWB >= 4 * PIXB ? ROWB : 0;
    }
}

__global__ __launch_bounds__(512, 2) TGT void swin_attn_kernel(
    const char* __restrict__ x, const char* __restrict__ wqkv, const char* __restrict__ wo,
    const unsigned* __restrict__ lscale, const unsigned* __restrict__ w1,
    const unsigned* __restrict__ w2, const unsigned* __restrict__ relc,
    const int* __restrict__ relidx, const char* __restrict__ mask, char* __restrict__ out) {
    __shared__ __attribute__((aligned(16))) char smem[LDS_BYTES];
    lds_ptr L = (lds_ptr)smem;
    const int tid = __builtin_amdgcn_workitem_id_x();
    const int lane = tid & 63;
    const int wid = __builtin_amdgcn_readfirstlane(tid >> 6);
    const int hp = wid & 1;
    const int pair = __builtin_amdgcn_workgroup_id_x() * 4 + (wid >> 1);
    const int nun = pair < 868 ? 3 : 2;
    const int slot = lane & 31, hh = lane >> 5;
    const int own = hp ? 25 : 0, oth = hp ? 0 : 25;
    unsigned la, lb;
    {
        const int ta = slot < 24 ? own + slot : 24, tb = slot < 24 ? oth + slot : 24;
        const int tya = (ta * 37) >> 8, txa = ta - tya * 7, tyb = (tb * 37) >> 8, txb = tb - tyb * 7;
        la = tya * ROWB + txa * PIXB + hh * 16;
        lb = tyb * ROWB + txb * PIXB + hh * 16;
    }

    u32x4 wreg[7];
    const int wr0 = tid / 36, wj = tid - wr0 * 36;
#pragma unroll
    for (int i = 0; i < 7; ++i) {
        unsigned c = tid + 504 * i;
        c = c < 3456 ? c : 3455;
        wreg[i] = *(const u32x4*)(wqkv + c * 16);
    }
    u32x4 woreg[3];
    {
        unsigned t = tid < 384 ? tid : 383;
#pragma unroll
        for (int i = 0; i < 3; ++i) woreg[i] = *(const u32x4*)(wo + (t * 48 + i * 16));
    }
    const int ehp = wid >> 2, eqt = (wid >> 1) & 1, ekt = wid & 1;
    i32x4 ridx[3];
    int ridx1;
    {
        int eown = ehp ? 25 : 0, eoth = ehp ? 0 : 25;
        unsigned q = slot < 24 ? (eqt ? eoth : eown) + slot : 24;
        unsigned kb = (ekt ? eoth : eown) + 4 * hh;
#pragma unroll
        for (int i = 0; i < 3; ++i) ridx[i] = *(const i32x4_a4*)(relidx + (q * 49 + kb + 8 * i));
        ridx1 = relidx[q * 49 + 24];
    }
    const int ct = tid - 320;
    unsigned rc = relc[(unsigned)(ct < 0 ? 0 : (ct > 168 ? 168 : ct))];

    int wy, wx;
    win_of(pair, 0, wy, wx);
    unsigned offa, offb;
    tok_offs(wy, wx, la, lb, offa, offb);
    bf16x8 xa[6], xb[6];
#pragma unroll
    for (int s = 0; s < 6; ++s) { xa[s] = *(const bf16x8*)(x + (offa + s * 32)); xb[s] = *(const bf16x8*)(x + (offb + s * 32)); }
    u32x4 mreg[5];
    {
        const char* mw = mask + (size_t)(wy * NWIN + wx) * MASKB;
        const unsigned l16 = lane * 16;
#pragma unroll
        for (int i = 0; i < 4; ++i) mreg[i] = *(const u32x4_a2*)(mw + (l16 + i * 1024));
        mreg[4] = *(const u32x4_a2*)(mw + (l16 + 4096 < MASKB - 16 ? l16 + 4096 : MASKB - 16));
    }

    float tau[4];
#pragma unroll
    for (int h = 0; h < 4; ++h) {
        unsigned lw = lscale[h >> 1];
        float ls = (h & 1) ? bfhi(lw) : bflo(lw);
        ls = bfr(__builtin_fminf(__builtin_fmaxf(ls, -100.f), 4.6052f));
        tau[h] = uni(bfr(__builtin_amdgcn_exp2f(ls * LOG2E)));
    }
    unsigned w1r[16], w2r[32];
#pragma unroll
    for (int j = 0; j < 16; ++j) w1r[j] = w1[j];
#pragma unroll
    for (int j = 0; j < 32; ++j) w2r[j] = w2[j];
    if (tid < 504) {
        int dst;
        if (wj < 12) { int h2 = wj / 6, g8 = 6 + wj - h2 * 6; dst = h2 * 30720 + (g8 >> 2) * 6144 + (g8 & 3) * 16; }
        else if (wj < 24) { int j = wj - 12, h2 = j / 6, g8 = j - h2 * 6; dst = h2 * 30720 + (g8 >> 2) * 6144 + (g8 & 3) * 16; }
        else { int j = wj - 24, hd = j / 3, cc = j - hd * 3; dst = (hd >> 1) * 30720 + (3 + (hd & 1)) * 6144 + cc * 16; }
        dst += W_OFF + wr0 * 64;
#pragma unroll
        for (int i = 0; i < 7; ++i)
            if (wr0 + 14 * i < 96) *(LDS_AS u32x4*)(L + dst + i * 14 * 64) = wreg[i];
    }
    if (tid < 384) {
        int hd = tid / 96, k = tid - hd * 96;
        *(LDS_AS u32x4*)(L + W_OFF + (hd >> 1) * 30720 + (3 + (hd & 1)) * 6144 + k * 64 + 48) = u32x4{0, 0, 0, 0};
    }
    if (tid < 16) *(LDS_AS unsigned*)(L + FL_OFF + tid * 4) = 0;
    if (tid >= 16 && tid < 24) *(LDS_AS float*)(L + FL_OFF + tid * 4) = tid < 20 ? 0.f : 1.f;
    {
        float v[24];
        float s = 0.f;
#pragma unroll
        for (int i = 0; i < 3; ++i)
#pragma unroll
            for (int j = 0; j < 4; ++j) { v[8 * i + 2 * j] = bflo(woreg[i][j]); v[8 * i + 2 * j + 1] = bfhi(woreg[i][j]); }
#pragma unroll
        for (int i = 0; i < 24; ++i) s += v[i];
        s += asf(__builtin_amdgcn_update_dpp(0, asu(s), 0xB1, 0xf, 0xf, true));
        s += asf(__builtin_amdgcn_update_dpp(0, asu(s), 0x4E, 0xf, 0xf, true));
        s *= (1.f / 96.f);
        if (tid < 384) {
            int k = tid >> 2, part = tid & 3;
#pragma unroll
            for (int i = 0; i < 3; ++i) {
                int j = 3 * part + i;
                u32x4 o = {pkbf(v[8 * i] - s, v[8 * i + 1] - s), pkbf(v[8 * i + 2] - s, v[8 * i + 3] - s),
                           pkbf(v[8 * i + 4] - s, v[8 * i + 5] - s), pkbf(v[8 * i + 6] - s, v[8 * i + 7] - s)};
                *(LDS_AS u32x4*)(L + WO_OFF + (j >> 2) * 6144 + k * 64 + (j & 3) * 16) = o;
            }
        }
    }
    if (wid >= 5) {
        float c0 = bflo(rc), c1 = bfhi(rc);
        float z[4] = {0.f, 0.f, 0.f, 0.f};
#pragma unroll
        for (int j = 0; j < 16; ++j) {
            float a0 = (j & 1) ? bfhi(w1r[j >> 1]) : bflo(w1r[j >> 1]);
            float a1 = (j & 1) ? bfhi(w1r[8 + (j >> 1)]) : bflo(w1r[8 + (j >> 1)]);
            float h = bfr(__builtin_fmaf(c1, a1, c0 * a0));
            h = h > 0.f ? h : h * 0.f;
            z[0] = __builtin_fmaf(h, bflo(w2r[2 * j]), z[0]);
            z[1] = __builtin_fmaf(h, bfhi(w2r[2 * j]), z[1]);
            z[2] = __builtin_fmaf(h, bflo(w2r[2 * j + 1]), z[2]);
            z[3] = __builtin_fmaf(h, bfhi(w2r[2 * j + 1]), z[3]);
        }
        f32x4 o;
#pragma unroll
        for (int e = 0; e < 4; ++e) {
            float sg = bfr(__builtin_amdgcn_rcpf(1.f + __builtin_amdgcn_exp2f(-LOG2E * bfr(z[e]))));
            o[e] = (16.f * sg - 16.f - tau[e]) * LOG2E;
        }
        if (ct < 169) *(LDS_AS f32x4*)(L + TB_OFF + ct * 16) = o;
    }
    wg_barrier();
    {
#pragma unroll
        for (int i = 0; i < 3; ++i) {
            f32x2 t0 = *(LDS_AS f32x2*)(L + TB_OFF + ridx[i][0] * 16 + ehp * 8);
            f32x2 t1 = *(LDS_AS f32x2*)(L + TB_OFF + ridx[i][1] * 16 + ehp * 8);
            f32x2 t2 = *(LDS_AS f32x2*)(L + TB_OFF + ridx[i][2] * 16 + ehp * 8);
            f32x2 t3 = *(LDS_AS f32x2*)(L + TB_OFF + ridx[i][3] * 16 + ehp * 8);
#pragma unroll
            for (int e = 0; e < 2; ++e)
                *(LDS_AS f32x4*)(L + BI_OFF + (((2 * ehp + e) * 2 + eqt) * 6 + 3 * ekt + i) * 1024 + lane * 16) = f32x4{t0[e], t1[e], t2[e], t3[e]};
        }
        f32x2 t = *(LDS_AS f32x2*)(L + TB_OFF + ridx1 * 16 + ehp * 8);
        float tv = ekt ? t[1] : t[0];
        *(LDS_AS float*)(L + BS_OFF + ((2 * ehp + ekt) * 2 + eqt) * 256 + lane * 4) = hh ? -__builtin_inff() : tv;
    }
    wg_barrier();

    const int g16 = (lane >> 4) & 1, i16 = lane & 15, q4 = i16 >> 2, p4 = i16 & 3;
    const lds_ptr wp = L + W_OFF + hp * 30720 + (8 * hh + q4) * 64 + g16 * 32 + p4 * 8;
    const lds_ptr wop = L + WO_OFF + (4 * hh + q4) * 64 + g16 * 32 + (((p4 & 1) << 1) | (p4 >> 1)) * 8;
    const lds_ptr wop_own = wop + hp * 3072, wop_oth = wop + (1 - hp) * 3072;
    const lds_ptr bp = L + BI_OFF + hp * 24576 + lane * 16;
    const lds_ptr bsp = L + BS_OFF + hp * 1024 + lane * 4;
    const lds_ptr exw = L + EX_OFF + wid * 3072 + lane * 16;
    const lds_ptr exr = L + EX_OFF + (wid ^ 1) * 3072 + lane * 16;
    const lds_ptr onep = L + FL_OFF + 64 + (slot >= 24 ? 16 : 0);
    volatile LDS_AS unsigned* myfl = (volatile LDS_AS unsigned*)(L + FL_OFF + wid * 8);
    volatile LDS_AS unsigned* pfl = (volatile LDS_AS unsigned*)(L + FL_OFF + (wid ^ 1) * 8);
    const float t0 = hp ? tau[2] : tau[0], t1 = hp ? tau[3] : tau[1];
    const float c0 = uni(t0 * LOG2E), c1 = uni(t1 * LOG2E);
    const bool tau_bad = !(t0 <= 24.f && t1 <= 24.f);
    const bool st_ok = slot < 24 + (1 - hp);

    bool masked;
    {
        unsigned mor = 0;
#pragma unroll
        for (int i = 0; i < 5; ++i) mor |= mreg[i][0] | mreg[i][1] | mreg[i][2] | mreg[i][3];
        masked = __builtin_amdgcn_ballot_w64((mor & 0x7fff7fffu) != 0) != 0;
    }

    for (int u = 0; u < nun; ++u) {
        const bool slow_sm = tau_bad || masked;
        const bool more = u + 1 < nun;
        QKV t;
        float mn = 1.f, mx = 1.f;
        qk_phase<false>(xa, xb, wp, c0, c1, t, mn, mx);
        v_phase(xa, xb, wp, onep, t);
        if (__builtin_amdgcn_ballot_w64(!(mn >= 0.00390625f && mx <= 32768.f)) != 0) {
            asm volatile("" ::: "memory");
            qk_phase<true>(xa, xb, wp, c0, c1, t, mn, mx);
        }

        const unsigned soff = offa;
        const char* mwin = mask + (size_t)(wy * NWIN + wx) * MASKB;
        if (more) {
            win_of(pair, u + 1, wy, wx);
            const char* mw = mask + (size_t)(wy * NWIN + wx) * MASKB;
            const unsigned l16 = lane_id() * 16;
#pragma unroll
            for (int i = 0; i < 4; ++i) mreg[i] = *(const u32x4_a2*)(mw + (l16 + i * 1024));
            mreg[4] = *(const u32x4_a2*)(mw + (l16 + 4096 < MASKB - 16 ? l16 + 4096 : MASKB - 16));
        } else {
#pragma unroll
            for (int i = 0; i < 5; ++i) asm volatile("" : "=v"(mreg[i]));
        }

        unsigned ox[12], oo[12];
        if (!slow_sm) {
            attn_one<false>(t, t.Qb[0], 0, bp, 6144, bsp, 256, nullptr, ox);
            attn_one<false>(t, t.Qb[1], 1, bp, 12288 + 6144, bsp, 512 + 256, nullptr, ox + 6);
        } else {
            unsigned mk[13];
            const int ln = lane_id(), sl = ln & 31;
            load_mask_rows(mwin, sl < 24 ? oth + sl : 24, own, oth, ln >> 5, mk);
            attn_one<true>(t, t.Qb[0], 0, bp, 6144, bsp, 256, mk, ox);
            __builtin_amdgcn_sched_barrier(0);
            attn_one<true>(t, t.Qb[1], 1, bp, 12288 + 6144, bsp, 512 + 256, mk, ox + 6);
        }
        while (pfl[1] < (unsigned)u) __builtin_amdgcn_s_sleep(1);
        asm volatile("" ::: "memory");
#pragma unroll
        for (int i = 0; i < 3; ++i) *(LDS_AS u32x4*)(exw + i * 1024) = u32x4{ox[4 * i], ox[4 * i + 1], ox[4 * i + 2], ox[4 * i + 3]};
        asm volatile("" ::: "memory");
        myfl[0] = u + 1;
        asm volatile("" ::: "memory");
        if (more) {
            unsigned mor = 0;
#pragma unroll
            for (int i = 0; i < 5; ++i) mor |= mreg[i][0] | mreg[i][1] | mreg[i][2] | mreg[i][3];
            masked = __builtin_amdgcn_ballot_w64((mor & 0x7fff7fffu) != 0) != 0;
            tok_offs(wy, wx, la, lb, offa, offb);
#pragma unroll
            for (int s = 0; s < 6; ++s) { xa[s] = *(const bf16x8*)(x + (offa + s * 32)); xb[s] = *(const bf16x8*)(x + (offb + s * 32)); }
        } else {
#pragma unroll
            for (int s = 0; s < 6; ++s) { asm volatile("" : "=v"(xa[s])); asm volatile("" : "=v"(xb[s])); }
        }
        if (!slow_sm) {
            attn_one<false>(t, t.Qa[0], 0, bp, 0, bsp, 0, nullptr, oo);
            attn_one<false>(t, t.Qa[1], 1, bp, 12288, bsp, 512, nullptr, oo + 6);
        } else {
            unsigned mk[13];
            const int ln = lane_id(), sl = ln & 31;
            load_mask_rows(mwin, sl < 24 ? own + sl : 24, own, oth, ln >> 5, mk);
            attn_one<true>(t, t.Qa[0], 0, bp, 0, bsp, 0, mk, oo);
            __builtin_amdgcn_sched_barrier(0);
            attn_one<true>(t, t.Qa[1], 1, bp, 12288, bsp, 512, mk, oo + 6);
        }
        while (pfl[0] < (unsigned)(u + 1)) __builtin_amdgcn_s_sleep(1);
        asm volatile("" ::: "memory");
        u32x4 op[3];
#pragma unroll
        for (int i = 0; i < 3; ++i) op[i] = *(LDS_AS u32x4*)(exr + i * 1024);
        asm volatile("" ::: "memory");
        myfl[1] = u + 1;
        asm volatile("" ::: "memory");

        f32x16 y[3];
#pragma unroll
        for (int m = 0; m < 3; ++m) {
            f32x16 acc = {};
#pragma unroll
            for (int s = 0; s < 3; ++s) {
                bf16x8 w = trfrag(wop_own, m * 6144 + s * 1024, m * 6144 + s * 1024 + 512);
                u32x4 b = {oo[4 * s], oo[4 * s + 1], oo[4 * s + 2], oo[4 * s + 3]};
                acc = MFMA_BF(w, __builtin_bit_cast(bf16x8, b), acc);
            }
#pragma unroll
            for (int s = 0; s < 3; ++s) {
                bf16x8 w = trfrag(wop_oth, m * 6144 + s * 1024, m * 6144 + s * 1024 + 512);
                acc = MFMA_BF(w, __builtin_bit_cast(bf16x8, op[s]), acc);
            }
            y[m] = acc;
        }
        float s0 = 0.f, s1 = 0.f, s2 = 0.f;
#pragma unroll
        for (int r = 0; r < 16; ++r) {
            s0 = __builtin_fmaf(y[0][r], y[0][r], s0);
            s1 = __builtin_fmaf(y[1][r], y[1][r], s1);
            s2 = __builtin_fmaf(y[2][r], y[2][r], s2);
        }
        float ss = s0 + s1 + s2;
        auto r2 = __builtin_amdgcn_permlane32_swap(asu(ss), asu(ss), false, false);
        ss = asf(r2[0]) + asf(r2[1]);
        const float rs = __builtin_amdgcn_rsqf(__builtin_fmaf(ss, 1.f / 96.f, 1e-5f));
        if (st_ok) {
#pragma unroll
            for (int m = 0; m < 3; ++m)
#pragma unroll
                for (int v = 0; v < 2; ++v) {
                    u32x4 o = {pkbf(y[m][8 * v] * rs, y[m][8 * v + 1] * rs), pkbf(y[m][8 * v + 2] * rs, y[m][8 * v + 3] * rs),
                               pkbf(y[m][8 * v + 4] * rs, y[m][8 * v + 5] * rs), pkbf(y[m][8 * v + 6] * rs, y[m][8 * v + 7] * rs)};
                    *(u32x4*)(out + (soff + m * 64 + v * 32)) = o;
                }
        }
    }
}

extern "C" void kernel_launch(void* const* d_in, const int* in_sizes, int n_in,
                              void* d_out, int out_size, void* d_ws, size_t ws_size,
                              hipStream_t stream) {
    swin_attn_kernel<<<256, 512, 0, stream>>>(
        (const char*)d_in[0], (const char*)d_in[1], (const char*)d_in[2],
        (const unsigned*)d_in[3], (const unsigned*)d_in[4], (const unsigned*)d_in[5],
        (const unsigned*)d_in[6], (const int*)d_in[7], (const char*)d_in[8], (char*)d_out);
}
```

```cpp
#include <hip/hip_runtime.h>
#include <hip/hip_bf16.h>
#include <stdint.h>

#define DI __device__ __forceinline__

typedef __attribute__((ext_vector_type(4))) float f32x4;
typedef __attribute__((ext_vector_type(2))) float f32x2;
typedef __attribute__((ext_vector_type(8))) __bf16 bf16x8;
typedef __attribute__((ext_vector_type(2))) __bf16 bf16x2;
typedef __attribute__((ext_vector_type(8))) _Float16 f16x8;
typedef __attribute__((ext_vector_type(2))) _Float16 f16x2;
typedef __attribute__((ext_vector_type(4))) unsigned u32x4;
typedef __attribute__((ext_vector_type(2))) unsigned u32x2;
typedef u32x2 u32x2_a2 __attribute__((aligned(2)));
#define LDS_AS __attribute__((address_space(3)))
#define LDSP(T, a) ((LDS_AS T*)(uintptr_t)(a))

constexpr int IMG = 378;
constexpr int NWIN = 54;
constexpr unsigned ROWB = IMG * 192;
constexpr unsigned XBYTES = IMG * ROWB;
constexpr float LOG2E = 1.4426950408889634f;

#define FAST_ASM \
    "s_load_dwordx16 s[76:91], s[72:73], 0x0\n" \
    "s_load_dwordx4 s[92:95], s[72:73], 0x40\n" \
    "s_getpc_b64 s[50:51]\n" \
    "s_lshr_b32 s60, s74, 3\n" \
    "s_and_b32 s60, s60, 31\n" \
    "v_lshrrev_b32 v246, 4, v0\n" \
    "v_lshl_add_u32 v246, v246, 5, s60\n" \
    "v_min_u32 v246, 559, v246\n" \
    "v_lshlrev_b32 v246, 6, v246\n" \
    "global_load_dword v239, v246, s[50:51]\n" \
    "v_readfirstlane_b32 s75, v0\n" \
    "v_mov_b32 v243, v0\n" \
    "v_and_b32 v240, 63, v0\n" \
    "v_and_b32 v241, 15, v0\n" \
    "v_bfe_u32 v242, v0, 4, 2\n" \
    "s_lshr_b32 s75, s75, 6\n" \
    "v_mul_u32_u24 v245, 1821, v243\n" \
    "v_lshrrev_b32 v245, 16, v245\n" \
    "v_mul_u32_u24 v244, 8640, v245\n" \
    "v_lshl_add_u32 v244, v243, 4, v244\n" \
    "v_lshlrev_b32 v249, 1, v242\n" \
    "s_cmp_lt_u32 s75, 4\n" \
    "s_cbranch_scc1 LproA%=\n" \
    "s_setprio 3\n" \
    "s_sub_u32 s98, s75, 4\n" \
    "v_and_b32 v250, 31, v240\n" \
    "v_lshrrev_b32 v251, 5, v240\n" \
    "s_lshl_b32 s60, s98, 8\n" \
    "v_lshl_add_u32 v246, v250, 2, s60\n" \
    "v_lshl_or_b32 v246, v251, 31, v246\n" \
    "v_lshlrev_b32 v247, 1, v241\n" \
    "v_and_b32 v248, 3, v240\n" \
    "v_lshlrev_b32 v248, 1, v248\n" \
    "v_lshl_or_b32 v248, v251, 5, v248\n" \
    "v_min_u32 v252, 48, v240\n" \
    "v_lshlrev_b32 v252, 2, v252\n" \
    "s_mul_i32 s36, s98, 784\n" \
    "s_add_u32 s37, s36, 3136\n" \
    "s_add_u32 s38, s36, 6272\n" \
    "s_movk_i32 s39, 9408\n" \
    "s_waitcnt lgkmcnt(0)\n" \
    "v_lshlrev_b32 v39, 4, v240\n" \
    "v_lshlrev_b32 v244, 2, v241\n" \
    "v_add_u32 v244, 148736, v244\n" \
    "v_mov_b32 v245, 0\n" \
    "ds_write_b32 v244, v245\n" \
    "s_mov_b32 s64, s82\n" \
    "s_and_b32 s65, s83, 0xffff\n" \
    "s_mov_b32 s66, 12\n" \
    "s_mov_b32 s67, 0x20000\n" \
    "buffer_load_ushort v249, v249, s[64:67], 0 offen\n" \
    "s_mov_b32 s60, s88\n" \
    "s_and_b32 s61, s89, 0xffff\n" \
    "s_mov_b32 s62, 676\n" \
    "s_mov_b32 s63, 0x20000\n" \
    "buffer_load_dword v56, v246, s[60:63], 0 offen\n" \
    "buffer_load_dword v58, v246, s[60:63], 0 offen offset:128\n" \
    "s_mov_b32 s64, s84\n" \
    "s_and_b32 s65, s85, 0xffff\n" \
    "s_mov_b32 s66, 68\n" \
    "s_mov_b32 s67, 0x20000\n" \
    "buffer_load_ushort v60, v247, s[64:67], 0 offen\n" \
    "buffer_load_ushort v61, v247, s[64:67], 0 offen offset:32\n" \
    "s_mov_b32 s60, s86\n" \
    "s_and_b32 s61, s87, 0xffff\n" \
    "s_mov_b32 s62, 132\n" \
    "s_mov_b32 s63, 0x20000\n" \
    "buffer_load_ushort v62, v248, s[60:63], 0 offen\n" \
    "buffer_load_ushort v63, v248, s[60:63], 0 offen offset:8\n" \
    "buffer_load_ushort v64, v248, s[60:63], 0 offen offset:16\n" \
    "buffer_load_ushort v65, v248, s[60:63], 0 offen offset:24\n" \
    "buffer_load_ushort v66, v248, s[60:63], 0 offen offset:64\n" \
    "buffer_load_ushort v67, v248, s[60:63], 0 offen offset:72\n" \
    "buffer_load_ushort v68, v248, s[60:63], 0 offen offset:80\n" \
    "buffer_load_ushort v69, v248, s[60:63], 0 offen offset:88\n" \
    "s_mov_b32 s64, s90\n" \
    "s_and_b32 s65, s91, 0xffff\n" \
    "s_mov_b32 s66, 9604\n" \
    "s_mov_b32 s67, 0x20000\n" \
    "buffer_load_dword v72, v252, s[64:67], s36 offen\n" \
    "buffer_load_dword v73, v252, s[64:67], s36 offen offset:196\n" \
    "buffer_load_dword v74, v252, s[64:67], s36 offen offset:392\n" \
    "buffer_load_dword v75, v252, s[64:67], s36 offen offset:588\n" \
    "buffer_load_dword v76, v252, s[64:67], s37 offen\n" \
    "buffer_load_dword v77, v252, s[64:67], s37 offen offset:196\n" \
    "buffer_load_dword v78, v252, s[64:67], s37 offen offset:392\n" \
    "buffer_load_dword v79, v252, s[64:67], s37 offen offset:588\n" \
    "buffer_load_dword v80, v252, s[64:67], s38 offen\n" \
    "buffer_load_dword v81, v252, s[64:67], s38 offen offset:196\n" \
    "buffer_load_dword v82, v252, s[64:67], s38 offen offset:392\n" \
    "buffer_load_dword v83, v252, s[64:67], s38 offen offset:588\n" \
    "buffer_load_dword v84, v252, s[64:67], s39 offen\n" \
    "s_waitcnt lgkmcnt(0)\n" \
    "s_barrier\n" \
    "s_waitcnt vmcnt(25)\n" \
    "v_lshlrev_b32 v52, 16, v249\n" \
    "s_mov_b32 s60, 0xc2c80000\n" \
    "v_mov_b32 v246, 0x40935dcc\n" \
    "v_med3_f32 v52, v52, s60, v246\n" \
    "v_cvt_pk_bf16_f32 v52, 0, v52\n" \
    "v_mul_f32 v52, 0x3fb8aa3b, v52\n" \
    "v_exp_f32 v52, v52\n" \
    "s_nop 0\n" \
    "v_cvt_pk_bf16_f32 v52, 0, v52\n" \
    "v_mul_f32 v52, 0x3fb8aa3b, v52\n" \
    "s_nop 1\n" \
    "v_readlane_b32 s36, v52, 0\n" \
    "v_readlane_b32 s37, v52, 16\n" \
    "v_readlane_b32 s38, v52, 32\n" \
    "v_readlane_b32 s39, v52, 48\n" \
    "v_mov_b32 v57, 0\n" \
    "v_mov_b32 v59, 0\n" \
    "v_cmp_eq_u32 vcc, 0, v251\n" \
    "v_mov_b32 v254, s38\n" \
    "v_mov_b32 v255, s39\n" \
    "v_mov_b32 v246, s36\n" \
    "v_mov_b32 v247, s37\n" \
    "v_cndmask_b32 v254, v254, v246, vcc\n" \
    "v_cndmask_b32 v255, v255, v247, vcc\n" \
    "s_waitcnt vmcnt(13)\n" \
    "v_lshl_or_b32 v60, v61, 16, v60\n" \
    "v_mov_b32 v61, 0\n" \
    "v_lshl_or_b32 v124, v63, 16, v62\n" \
    "v_lshl_or_b32 v125, v65, 16, v64\n" \
    "v_lshl_or_b32 v126, v67, 16, v66\n" \
    "v_lshl_or_b32 v127, v69, 16, v68\n" \
    "v_mfma_f32_32x32x8_bf16 v[92:107], v[60:61], v[56:57], 0\n" \
    "v_mfma_f32_32x32x8_bf16 v[108:123], v[60:61], v[58:59], 0\n" \
    "s_mul_i32 s60, s98, 1280\n" \
    "s_add_u32 s60, s60, 148992\n" \
    "v_lshlrev_b32 v246, 3, v251\n" \
    "v_mad_u32_u24 v246, v250, 20, v246\n" \
    "v_add_u32 v246, s60, v246\n" \
    "s_mov_b32 s60, 0x41b8aa3b\n" \
    "v_add_f32 v254, s60, v254\n" \
    "v_add_f32 v255, s60, v255\n" \
    "s_nop 2\n" \
    "v_max_f32 v92, 0, v92\n" \
    "v_max_f32 v93, 0, v93\n" \
    "v_max_f32 v94, 0, v94\n" \
    "v_max_f32 v95, 0, v95\n" \
    "v_max_f32 v96, 0, v96\n" \
    "v_max_f32 v97, 0, v97\n" \
    "v_max_f32 v98, 0, v98\n" \
    "v_max_f32 v99, 0, v99\n" \
    "v_cvt_pk_bf16_f32 v128, v92, v93\n" \
    "v_cvt_pk_bf16_f32 v129, v94, v95\n" \
    "v_cvt_pk_bf16_f32 v130, v96, v97\n" \
    "v_cvt_pk_bf16_f32 v131, v98, v99\n" \
    "v_max_f32 v108, 0, v108\n" \
    "v_max_f32 v109, 0, v109\n" \
    "v_max_f32 v110, 0, v110\n" \
    "v_max_f32 v111, 0, v111\n" \
    "v_max_f32 v112, 0, v112\n" \
    "v_max_f32 v113, 0, v113\n" \
    "v_max_f32 v114, 0, v114\n" \
    "v_max_f32 v115, 0, v115\n" \
    "v_cvt_pk_bf16_f32 v132, v108, v109\n" \
    "v_cvt_pk_bf16_f32 v133, v110, v111\n" \
    "v_cvt_pk_bf16_f32 v134, v112, v113\n" \
    "v_cvt_pk_bf16_f32 v135, v114, v115\n" \
    "v_mfma_f32_32x32x16_bf16 v[92:107], v[124:127], v[128:131], 0\n" \
    "s_nop 0\n" \
    "v_mfma_f32_32x32x16_bf16 v[108:123], v[124:127], v[132:135], 0\n" \
    "v_lshrrev_b32 v247, 2, v240\n" \
    "v_and_b32 v248, 3, v240\n" \
    "v_cmp_gt_u32 s[62:63], 48, v240\n" \
    "v_mul_u32_u24 v244, 9248, v248\n" \
    "v_lshl_add_u32 v244, v247, 8, v244\n" \
    "v_lshlrev_b32 v20, 8, v248\n" \
    "v_add_u32 v21, 36992, v20\n" \
    "v_cndmask_b32 v244, v21, v244, s[62:63]\n" \
    "v_cndmask_b32 v21, v248, v247, s[62:63]\n" \
    "v_and_b32 v21, 1, v21\n" \
    "v_lshlrev_b32 v21, 4, v21\n" \
    "s_lshl_b32 s61, s98, 6\n" \
    "s_add_u32 s61, s61, 74624\n" \
    "v_add3_u32 v12, v244, v21, s61\n" \
    "v_xor_b32 v13, 16, v12\n" \
    "v_mov_b32 v21, 1024\n" \
    "v_mov_b32 v22, 3072\n" \
    "v_cndmask_b32 v21, v21, v22, s[62:63]\n" \
    "v_lshl_add_u32 v20, v247, 4, v20\n" \
    "v_add_u32 v18, 114688, v20\n" \
    "v_lshlrev_b32 v23, 2, v248\n" \
    "v_add_u32 v23, 148992, v23\n" \
    "v_add_u32 v14, v12, v21\n" \
    "v_add_u32 v15, v13, v21\n" \
    "v_add_u32 v16, v14, v21\n" \
    "v_add_u32 v17, v15, v21\n" \
    "v_cndmask_b32 v92, v94, v92, vcc\n" \
    "v_cndmask_b32 v93, v95, v93, vcc\n" \
    "v_cndmask_b32 v108, v110, v108, vcc\n" \
    "v_cndmask_b32 v109, v111, v109, vcc\n" \
    "v_cvt_pk_bf16_f32 v92, 0, v92\n" \
    "v_cvt_pk_bf16_f32 v93, 0, v93\n" \
    "v_cvt_pk_bf16_f32 v108, 0, v108\n" \
    "v_cvt_pk_bf16_f32 v109, 0, v109\n" \
    "v_mul_f32 v92, 0xbfb8aa3b, v92\n" \
    "v_mul_f32 v93, 0xbfb8aa3b, v93\n" \
    "v_mul_f32 v108, 0xbfb8aa3b, v108\n" \
    "v_mul_f32 v109, 0xbfb8aa3b, v109\n" \
    "v_exp_f32 v92, v92\n" \
    "v_exp_f32 v93, v93\n" \
    "v_exp_f32 v108, v108\n" \
    "v_exp_f32 v109, v109\n" \
    "v_add_f32 v92, 1.0, v92\n" \
    "v_add_f32 v93, 1.0, v93\n" \
    "v_add_f32 v108, 1.0, v108\n" \
    "v_add_f32 v109, 1.0, v109\n" \
    "v_rcp_f32 v92, v92\n" \
    "v_rcp_f32 v93, v93\n" \
    "v_rcp_f32 v108, v108\n" \
    "v_rcp_f32 v109, v109\n" \
    "v_cvt_pk_bf16_f32 v92, 0, v92\n" \
    "v_cvt_pk_bf16_f32 v93, 0, v93\n" \
    "v_cvt_pk_bf16_f32 v108, 0, v108\n" \
    "v_cvt_pk_bf16_f32 v109, 0, v109\n" \
    "v_fma_f32 v92, v92, s60, -v254\n" \
    "v_fma_f32 v93, v93, s60, -v255\n" \
    "v_fma_f32 v108, v108, s60, -v254\n" \
    "v_fma_f32 v109, v109, s60, -v255\n" \
    "ds_write2_b32 v246, v92, v93 offset1:1\n" \
    "ds_write2_b32 v246, v108, v109 offset0:160 offset1:161\n" \
    "s_nop 1\n" \
    "v_mov_b32 v246, 148748\n" \
    "v_mov_b32 v247, 1\n" \
    "s_mov_b64 exec, 1\n" \
    "ds_add_u32 v246, v247\n" \
    "s_mov_b64 exec, -1\n" \
    "s_waitcnt vmcnt(0)\n" \
    "v_mul_u32_u24 v72, 20, v72\n" \
    "v_mul_u32_u24 v73, 20, v73\n" \
    "v_mul_u32_u24 v74, 20, v74\n" \
    "v_mul_u32_u24 v75, 20, v75\n" \
    "v_mul_u32_u24 v76, 20, v76\n" \
    "v_mul_u32_u24 v77, 20, v77\n" \
    "v_mul_u32_u24 v78, 20, v78\n" \
    "v_mul_u32_u24 v79, 20, v79\n" \
    "v_mul_u32_u24 v80, 20, v80\n" \
    "v_mul_u32_u24 v81, 20, v81\n" \
    "v_mul_u32_u24 v82, 20, v82\n" \
    "v_mul_u32_u24 v83, 20, v83\n" \
    "v_mul_u32_u24 v84, 20, v84\n" \
    "v_add_u32_dpp v140, v72, v23 quad_perm:[0,0,0,0] row_mask:0xf bank_mask:0xf\n" \
    "v_add_u32_dpp v141, v72, v23 quad_perm:[1,1,1,1] row_mask:0xf bank_mask:0xf\n" \
    "v_add_u32_dpp v142, v72, v23 quad_perm:[2,2,2,2] row_mask:0xf bank_mask:0xf\n" \
    "v_add_u32_dpp v143, v72, v23 quad_perm:[3,3,3,3] row_mask:0xf bank_mask:0xf\n" \
    "v_add_u32_dpp v144, v73, v23 quad_perm:[0,0,0,0] row_mask:0xf bank_mask:0xf\n" \
    "v_add_u32_dpp v145, v73, v23 quad_perm:[1,1,1,1] row_mask:0xf bank_mask:0xf\n" \
    "v_add_u32_dpp v146, v73, v23 quad_perm:[2,2,2,2] row_mask:0xf bank_mask:0xf\n" \
    "v_add_u32_dpp v147, v73, v23 quad_perm:[3,3,3,3] row_mask:0xf bank_mask:0xf\n" \
    "v_add_u32_dpp v148, v74, v23 quad_perm:[0,0,0,0] row_mask:0xf bank_mask:0xf\n" \
    "v_add_u32_dpp v149, v74, v23 quad_perm:[1,1,1,1] row_mask:0xf bank_mask:0xf\n" \
    "v_add_u32_dpp v150, v74, v23 quad_perm:[2,2,2,2] row_mask:0xf bank_mask:0xf\n" \
    "v_add_u32_dpp v151, v74, v23 quad_perm:[3,3,3,3] row_mask:0xf bank_mask:0xf\n" \
    "v_add_u32_dpp v152, v75, v23 quad_perm:[0,0,0,0] row_mask:0xf bank_mask:0xf\n" \
    "v_add_u32_dpp v153, v75, v23 quad_perm:[1,1,1,1] row_mask:0xf bank_mask:0xf\n" \
    "v_add_u32_dpp v154, v75, v23 quad_perm:[2,2,2,2] row_mask:0xf bank_mask:0xf\n" \
    "v_add_u32_dpp v155, v75, v23 quad_perm:[3,3,3,3] row_mask:0xf bank_mask:0xf\n" \
    "v_add_u32_dpp v156, v76, v23 quad_perm:[0,0,0,0] row_mask:0xf bank_mask:0xf\n" \
    "v_add_u32_dpp v157, v76, v23 quad_perm:[1,1,1,1] row_mask:0xf bank_mask:0xf\n" \
    "v_add_u32_dpp v158, v76, v23 quad_perm:[2,2,2,2] row_mask:0xf bank_mask:0xf\n" \
    "v_add_u32_dpp v159, v76, v23 quad_perm:[3,3,3,3] row_mask:0xf bank_mask:0xf\n" \
    "v_add_u32_dpp v160, v77, v23 quad_perm:[0,0,0,0] row_mask:0xf bank_mask:0xf\n" \
    "v_add_u32_dpp v161, v77, v23 quad_perm:[1,1,1,1] row_mask:0xf bank_mask:0xf\n" \
    "v_add_u32_dpp v162, v77, v23 quad_perm:[2,2,2,2] row_mask:0xf bank_mask:0xf\n" \
    "v_add_u32_dpp v163, v77, v23 quad_perm:[3,3,3,3] row_mask:0xf bank_mask:0xf\n" \
    "v_add_u32_dpp v164, v78, v23 quad_perm:[0,0,0,0] row_mask:0xf bank_mask:0xf\n" \
    "v_add_u32_dpp v165, v78, v23 quad_perm:[1,1,1,1] row_mask:0xf bank_mask:0xf\n" \
    "v_add_u32_dpp v166, v78, v23 quad_perm:[2,2,2,2] row_mask:0xf bank_mask:0xf\n" \
    "v_add_u32_dpp v167, v78, v23 quad_perm:[3,3,3,3] row_mask:0xf bank_mask:0xf\n" \
    "v_add_u32_dpp v168, v79, v23 quad_perm:[0,0,0,0] row_mask:0xf bank_mask:0xf\n" \
    "v_add_u32_dpp v169, v79, v23 quad_perm:[1,1,1,1] row_mask:0xf bank_mask:0xf\n" \
    "v_add_u32_dpp v170, v79, v23 quad_perm:[2,2,2,2] row_mask:0xf bank_mask:0xf\n" \
    "v_add_u32_dpp v171, v79, v23 quad_perm:[3,3,3,3] row_mask:0xf bank_mask:0xf\n" \
    "v_add_u32_dpp v172, v80, v23 quad_perm:[0,0,0,0] row_mask:0xf bank_mask:0xf\n" \
    "v_add_u32_dpp v173, v80, v23 quad_perm:[1,1,1,1] row_mask:0xf bank_mask:0xf\n" \
    "v_add_u32_dpp v174, v80, v23 quad_perm:[2,2,2,2] row_mask:0xf bank_mask:0xf\n" \
    "v_add_u32_dpp v175, v80, v23 quad_perm:[3,3,3,3] row_mask:0xf bank_mask:0xf\n" \
    "v_add_u32_dpp v176, v81, v23 quad_perm:[0,0,0,0] row_mask:0xf bank_mask:0xf\n" \
    "v_add_u32_dpp v177, v81, v23 quad_perm:[1,1,1,1] row_mask:0xf bank_mask:0xf\n" \
    "v_add_u32_dpp v178, v81, v23 quad_perm:[2,2,2,2] row_mask:0xf bank_mask:0xf\n" \
    "v_add_u32_dpp v179, v81, v23 quad_perm:[3,3,3,3] row_mask:0xf bank_mask:0xf\n" \
    "v_add_u32_dpp v180, v82, v23 quad_perm:[0,0,0,0] row_mask:0xf bank_mask:0xf\n" \
    "v_add_u32_dpp v181, v82, v23 quad_perm:[1,1,1,1] row_mask:0xf bank_mask:0xf\n" \
    "v_add_u32_dpp v182, v82, v23 quad_perm:[2,2,2,2] row_mask:0xf bank_mask:0xf\n" \
    "v_add_u32_dpp v183, v82, v23 quad_perm:[3,3,3,3] row_mask:0xf bank_mask:0xf\n" \
    "v_add_u32_dpp v184, v83, v23 quad_perm:[0,0,0,0] row_mask:0xf bank_mask:0xf\n" \
    "v_add_u32_dpp v185, v83, v23 quad_perm:[1,1,1,1] row_mask:0xf bank_mask:0xf\n" \
    "v_add_u32_dpp v186, v83, v23 quad_perm:[2,2,2,2] row_mask:0xf bank_mask:0xf\n" \
    "v_add_u32_dpp v187, v83, v23 quad_perm:[3,3,3,3] row_mask:0xf bank_mask:0xf\n" \
    "v_add_u32_dpp v188, v84, v23 quad_perm:[0,0,0,0] row_mask:0xf bank_mask:0xf\n" \
    "v_add_u32_dpp v189, v84, v23 quad_perm:[1,1,1,1] row_mask:0xf bank_mask:0xf\n" \
    "v_add_u32_dpp v190, v84, v23 quad_perm:[2,2,2,2] row_mask:0xf bank_mask:0xf\n" \
    "v_add_u32_dpp v191, v84, v23 quad_perm:[3,3,3,3] row_mask:0xf bank_mask:0xf\n" \
    "v_mov_b32 v246, 148748\n" \
    "s_branch Lwte%=\n" \
    "Lwt%=:\n" \
    "s_sleep 1\n" \
    "Lwte%=:\n" \
    "ds_read_b32 v247, v246\n" \
    "s_waitcnt lgkmcnt(0)\n" \
    "v_cmp_gt_u32 vcc, 4, v247\n" \
    "s_nop 1\n" \
    "s_cbranch_vccnz Lwt%=\n" \
    "s_setprio 0\n" \
    "ds_read_b32 v140, v140\n" \
    "ds_read_b32 v141, v141\n" \
    "ds_read_b32 v142, v142\n" \
    "ds_read_b32 v143, v143\n" \
    "ds_read_b32 v144, v144\n" \
    "ds_read_b32 v145, v145\n" \
    "ds_read_b32 v146, v146\n" \
    "ds_read_b32 v147, v147\n" \
    "ds_read_b32 v148, v148\n" \
    "ds_read_b32 v149, v149\n" \
    "ds_read_b32 v150, v150\n" \
    "ds_read_b32 v151, v151\n" \
    "s_cmp_lt_u32 s74, 156\n" \
    "s_cselect_b32 s60, 1, 0\n" \
    "s_mul_i32 s61, s74, 10\n" \
    "s_mul_i32 s62, s74, 12\n" \
    "s_sub_u32 s62, s62, 312\n" \
    "s_max_i32 s71, s61, s62\n" \
    "s_sub_u32 s53, 4, s60\n" \
    "s_sub_u32 s34, 12, s60\n" \
    "s_lshr_b32 s61, s75, 2\n" \
    "s_lshl_b32 s62, s61, 2\n" \
    "s_add_u32 s35, s75, s62\n" \
    "s_lshl_b32 s62, s60, s61\n" \
    "s_add_u32 s52, s71, s35\n" \
    "s_waitcnt lgkmcnt(8)\n" \
    "ds_write_b128 v12, v[140:143]\n" \
    "ds_read_b32 v152, v152\n" \
    "ds_read_b32 v153, v153\n" \
    "ds_read_b32 v154, v154\n" \
    "ds_read_b32 v155, v155\n" \
    "s_sub_u32 s52, s52, s62\n" \
    "s_sub_u32 s54, 2, s61\n" \
    "s_lshl_b32 s62, s60, 3\n" \
    "s_or_b32 s62, s62, s75\n" \
    "s_add_u32 s63, s74, 2760\n" \
    "s_cmp_eq_u32 s62, 8\n" \
    "s_cselect_b32 s54, 1, s54\n" \
    "s_cselect_b32 s52, s63, s52\n" \
    "s_min_u32 s63, s52, 2808\n" \
    "s_mul_i32 s61, s63, 19785\n" \
    "s_lshr_b32 s61, s61, 20\n" \
    "s_mul_i32 s61, s61, 436800\n" \
    "s_mul_i32 s56, s63, 1344\n" \
    "s_waitcnt lgkmcnt(9)\n" \
    "ds_write_b128 v13, v[144:147]\n" \
    "ds_read_b32 v156, v156\n" \
    "ds_read_b32 v157, v157\n" \
    "ds_read_b32 v158, v158\n" \
    "ds_read_b32 v159, v159\n" \
    "s_add_u32 s56, s56, s61\n" \
    "s_add_u32 s56, s56, 218304\n" \
    "s_mov_b32 s96, 71232\n" \
    "s_movk_i32 s97, 192\n" \
    "v_lshlrev_b32 v247, 4, v242\n" \
    "s_mov_b32 s99, s60\n" \
    "v_add_u32 v246, 0, v241\n" \
    "v_mul_u32_u24 v248, 37, v246\n" \
    "v_lshrrev_b32 v248, 8, v248\n" \
    "v_mad_u32_u24 v246, v246, s97, v247\n" \
    "v_mad_u32_u24 v36, v248, s96, v246\n" \
    "v_add_u32 v246, 16, v241\n" \
    "v_mul_u32_u24 v248, 37, v246\n" \
    "s_waitcnt lgkmcnt(10)\n" \
    "ds_write_b128 v12, v[148:151] offset:32\n" \
    "ds_read_b32 v160, v160\n" \
    "ds_read_b32 v161, v161\n" \
    "ds_read_b32 v162, v162\n" \
    "ds_read_b32 v163, v163\n" \
    "v_lshrrev_b32 v248, 8, v248\n" \
    "v_mad_u32_u24 v246, v246, s97, v247\n" \
    "v_mad_u32_u24 v37, v248, s96, v246\n" \
    "v_add_u32 v246, 32, v241\n" \
    "v_mul_u32_u24 v248, 37, v246\n" \
    "v_lshrrev_b32 v248, 8, v248\n" \
    "v_mad_u32_u24 v246, v246, s97, v247\n" \
    "v_mad_u32_u24 v38, v248, s96, v246\n" \
    "s_mov_b32 s40, s76\n" \
    "s_and_b32 s41, s77, 0xffff\n" \
    "s_mov_b32 s42, 27433728\n" \
    "s_mov_b32 s43, 0x20000\n" \
    "v_cmp_lt_u32 vcc, 4, v241\n" \
    "s_waitcnt lgkmcnt(10)\n" \
    "ds_write_b128 v13, v[152:155] offset:32\n" \
    "ds_read_b32 v164, v164\n" \
    "ds_read_b32 v165, v165\n" \
    "ds_read_b32 v166, v166\n" \
    "ds_read_b32 v167, v167\n" \
    "v_add_u32 v246, s71, v241\n" \
    "v_mov_b32 v248, s99\n" \
    "v_cndmask_b32 v244, 0, v248, vcc\n" \
    "v_sub_u32 v246, v246, v244\n" \
    "s_add_u32 s60, s74, 2760\n" \
    "v_sub_u32 v246, v246, v248\n" \
    "v_lshl_or_b32 v248, v248, 4, v241\n" \
    "v_cmp_eq_u32 vcc, 16, v248\n" \
    "v_mov_b32 v244, s60\n" \
    "s_movk_i32 s61, 2809\n" \
    "v_cndmask_b32 v246, v246, v244, vcc\n" \
    "v_cmp_le_u32 s[64:65], s61, v246\n" \
    "v_cmp_ne_u32 vcc, 20, v248\n" \
    "s_waitcnt lgkmcnt(10)\n" \
    "ds_write_b128 v14, v[156:159]\n" \
    "ds_read_b32 v168, v168\n" \
    "ds_read_b32 v169, v169\n" \
    "ds_read_b32 v170, v170\n" \
    "ds_read_b32 v171, v171\n" \
    "v_cmp_gt_u32 s[62:63], 12, v241\n" \
    "v_mul_u32_u24 v244, 19785, v246\n" \
    "s_and_b64 vcc, vcc, s[62:63]\n" \
    "v_lshrrev_b32 v244, 20, v244\n" \
    "v_mul_u32_u24 v246, 1344, v246\n" \
    "v_mul_u32_u24 v244, 436800, v244\n" \
    "s_mov_b32 s60, 654912\n" \
    "v_add3_u32 v246, v246, v244, s60\n" \
    "s_sub_u32 s60, s74, 49\n" \
    "s_mul_i32 s61, s60, 1344\n" \
    "s_add_u32 s61, s61, 146880\n" \
    "s_cmp_eq_u32 s60, 53\n" \
    "s_cselect_b32 s61, 145536, s61\n" \
    "s_waitcnt lgkmcnt(10)\n" \
    "ds_write_b128 v15, v[160:163]\n" \
    "ds_read_b32 v172, v172\n" \
    "ds_read_b32 v173, v173\n" \
    "ds_read_b32 v174, v174\n" \
    "ds_read_b32 v175, v175\n" \
    "s_sub_u32 s66, s60, 54\n" \
    "s_mul_i32 s66, s66, 508032\n" \
    "s_add_u32 s66, s66, 653568\n" \
    "s_cmp_lt_i32 s60, 54\n" \
    "s_cselect_b32 s61, s61, s66\n" \
    "v_mov_b32 v244, s61\n" \
    "v_cndmask_b32 v246, v246, v244, s[64:65]\n" \
    "v_mov_b32 v244, 0x80000000\n" \
    "v_cndmask_b32 v246, v244, v246, vcc\n" \
    "v_lshlrev_b32 v244, 2, v241\n" \
    "v_add_u32 v244, 148800, v244\n" \
    "ds_write_b32 v244, v246\n" \
    "v_add_u32 v253, v246, v247\n" \
    "s_waitcnt lgkmcnt(10)\n" \
    "ds_write_b128 v14, v[164:167] offset:32\n" \
    "ds_read_b32 v176, v176\n" \
    "ds_read_b32 v177, v177\n" \
    "ds_read_b32 v178, v178\n" \
    "ds_read_b32 v179, v179\n" \
    "buffer_load_dwordx4 v[228:231], v253, s[40:43], 0 offen\n" \
    "buffer_load_dwordx4 v[232:235], v253, s[40:43], 0 offen offset:64\n" \
    "buffer_load_dwordx4 v[236:239], v253, s[40:43], 0 offen offset:128\n" \
    "s_mov_b32 s48, s94\n" \
    "s_and_b32 s49, s95, 0xffff\n" \
    "s_mov_b32 s50, 27433728\n" \
    "s_mov_b32 s51, 0x20000\n" \
    "v_add_u32 v40, 56192, v39\n" \
    "v_and_b32 v246, 1, v242\n" \
    "v_xor_b32 v246, v246, v240\n" \
    "v_lshlrev_b32 v246, 4, v246\n" \
    "v_add_u32 v41, 74624, v246\n" \
    "v_lshrrev_b32 v246, 2, v241\n" \
    "s_waitcnt lgkmcnt(10)\n" \
    "ds_write_b128 v15, v[168:171] offset:32\n" \
    "ds_read_b32 v180, v180\n" \
    "ds_read_b32 v181, v181\n" \
    "ds_read_b32 v182, v182\n" \
    "ds_read_b32 v183, v183\n" \
    "v_lshlrev_b32 v247, 4, v242\n" \
    "v_lshl_add_u32 v248, v246, 8, v247\n" \
    "v_add_u32 v42, 114688, v248\n" \
    "v_lshlrev_b32 v43, 2, v241\n" \
    "s_mul_i32 s60, s35, 2064\n" \
    "s_add_u32 s60, s60, 115712\n" \
    "v_lshl_add_u32 v54, v241, 3, s60\n" \
    "v_lshl_add_u32 v247, v241, 4, v247\n" \
    "v_add_u32 v247, s60, v247\n" \
    "v_mov_b32 v248, 196608\n" \
    "v_mov_b32 v244, 0x3c003c00\n" \
    "v_cmp_eq_u32 vcc, 0, v241\n" \
    "v_cmp_eq_u32 s[60:61], 0, v246\n" \
    "s_waitcnt lgkmcnt(10)\n" \
    "ds_write_b128 v16, v[172:175]\n" \
    "ds_read_b32 v184, v184\n" \
    "ds_read_b32 v185, v185\n" \
    "ds_read_b32 v186, v186\n" \
    "ds_read_b32 v187, v187\n" \
    "s_nop 0\n" \
    "v_cndmask_b32 v44, v248, v247, vcc\n" \
    "v_cndmask_b32 v48, 0, v244, s[60:61]\n" \
    "v_cmp_eq_u32 vcc, 4, v241\n" \
    "v_cmp_eq_u32 s[60:61], 1, v246\n" \
    "s_nop 0\n" \
    "v_cndmask_b32 v45, v248, v247, vcc\n" \
    "v_cndmask_b32 v49, 0, v244, s[60:61]\n" \
    "v_cmp_eq_u32 vcc, 8, v241\n" \
    "v_cmp_eq_u32 s[60:61], 2, v246\n" \
    "s_nop 0\n" \
    "v_cndmask_b32 v46, v248, v247, vcc\n" \
    "v_cndmask_b32 v50, 0, v244, s[60:61]\n" \
    "s_waitcnt lgkmcnt(10)\n" \
    "ds_write_b128 v17, v[176:179]\n" \
    "ds_read_b32 v188, v188\n" \
    "ds_read_b32 v189, v189\n" \
    "ds_read_b32 v190, v190\n" \
    "ds_read_b32 v191, v191\n" \
    "v_cmp_eq_u32 vcc, 12, v241\n" \
    "v_cmp_eq_u32 s[60:61], 3, v246\n" \
    "s_nop 0\n" \
    "v_cndmask_b32 v47, v248, v247, vcc\n" \
    "v_cndmask_b32 v51, 0, v244, s[60:61]\n" \
    "s_mov_b32 s67, 0x3b800000\n" \
    "s_mov_b32 s68, 0x47000000\n" \
    "s_mov_b32 s69, 0x7fff7fff\n" \
    "s_mov_b32 s70, 0x3f803f80\n" \
    "s_mov_b32 s58, 0x33333333\n" \
    "s_mov_b32 s59, 0x33333333\n" \
    "s_mov_b32 s55, 1\n" \
    "v_mov_b32 v95, 0\n" \
    "s_waitcnt lgkmcnt(10)\n" \
    "ds_write_b128 v16, v[180:183] offset:32\n" \
    "v_mov_b32 v96, 0\n" \
    "v_mov_b32 v103, 0\n" \
    "v_mov_b32 v104, 0\n" \
    "v_mov_b32 v111, 0\n" \
    "v_mov_b32 v112, 0\n" \
    "v_mov_b32 v119, 0\n" \
    "v_mov_b32 v120, 0\n" \
    "v_mov_b32 v127, 0\n" \
    "v_mov_b32 v128, 0\n" \
    "v_mov_b32 v135, 0\n" \
    "v_mov_b32 v136, 0\n" \
    "s_waitcnt lgkmcnt(6)\n" \
    "ds_write_b128 v17, v[184:187] offset:32\n" \
    "s_waitcnt lgkmcnt(2)\n" \
    "ds_write_b128 v18, v[188:191]\n" \
    "s_setprio 3\n" \
    "s_cmp_lg_u32 s98, 3\n" \
    "s_cbranch_scc1 LproX%=\n" \
    "v_mov_b32 v246, 148740\n" \
    "v_mov_b32 v247, 1\n" \
    "s_mov_b64 exec, 1\n" \
    "ds_add_u32 v246, v247\n" \
    "s_mov_b64 exec, -1\n" \
    "s_mov_b32 s44, s80\n" \
    "s_and_b32 s45, s81, 0xffff\n" \
    "s_mov_b32 s46, 18432\n" \
    "s_mov_b32 s47, 0x20000\n" \
    "v_lshrrev_b32 v246, 2, v241\n" \
    "v_and_b32 v247, 1, v241\n" \
    "v_lshl_or_b32 v246, v246, 1, v247\n" \
    "v_and_b32 v247, 2, v241\n" \
    "v_lshl_add_u32 v246, v247, 2, v246\n" \
    "v_lshlrev_b32 v247, 4, v242\n" \
    "v_mad_u32_u24 v248, v246, s97, v247\n" \
    "v_mad_u32_u24 v247, v241, s97, v247\n" \
    "s_movk_i32 s57, 0\n" \
    "buffer_load_dwordx4 v[140:143], v247, s[44:47], s57 offen\n" \
    "buffer_load_dwordx4 v[144:147], v247, s[44:47], s57 offen offset:64\n" \
    "buffer_load_dwordx4 v[148:151], v247, s[44:47], s57 offen offset:128\n" \
    "s_movk_i32 s57, 3072\n" \
    "buffer_load_dwordx4 v[152:155], v248, s[44:47], s57 offen\n" \
    "buffer_load_dwordx4 v[156:159], v248, s[44:47], s57 offen offset:64\n" \
    "buffer_load_dwordx4 v[160:163], v248, s[44:47], s57 offen offset:128\n" \
    "s_movk_i32 s57, 6144\n" \
    "buffer_load_dwordx4 v[164:167], v247, s[44:47], s57 offen\n" \
    "buffer_load_dwordx4 v[168:171], v247, s[44:47], s57 offen offset:64\n" \
    "buffer_load_dwordx4 v[172:175], v247, s[44:47], s57 offen offset:128\n" \
    "s_movk_i32 s57, 9216\n" \
    "buffer_load_dwordx4 v[176:179], v247, s[44:47], s57 offen\n" \
    "buffer_load_dwordx4 v[180:183], v247, s[44:47], s57 offen offset:64\n" \
    "buffer_load_dwordx4 v[184:187], v247, s[44:47], s57 offen offset:128\n" \
    "s_movk_i32 s57, 12288\n" \
    "buffer_load_dwordx4 v[188:191], v248, s[44:47], s57 offen\n" \
    "buffer_load_dwordx4 v[192:195], v248, s[44:47], s57 offen offset:64\n" \
    "buffer_load_dwordx4 v[196:199], v248, s[44:47], s57 offen offset:128\n" \
    "s_movk_i32 s57, 15360\n" \
    "buffer_load_dwordx4 v[200:203], v247, s[44:47], s57 offen\n" \
    "buffer_load_dwordx4 v[204:207], v247, s[44:47], s57 offen offset:64\n" \
    "buffer_load_dwordx4 v[208:211], v247, s[44:47], s57 offen offset:128\n" \
    "buffer_load_dwordx4 v[0:3], v36, s[40:43], s56 offen \n" \
    "buffer_load_dwordx4 v[12:15], v37, s[40:43], s56 offen \n" \
    "buffer_load_dwordx4 v[24:27], v38, s[40:43], s56 offen \n" \
    "buffer_load_dwordx4 v[4:7], v36, s[40:43], s56 offen offset:64 \n" \
    "buffer_load_dwordx4 v[16:19], v37, s[40:43], s56 offen offset:64 \n" \
    "buffer_load_dwordx4 v[28:31], v38, s[40:43], s56 offen offset:64 \n" \
    "buffer_load_dwordx4 v[8:11], v36, s[40:43], s56 offen offset:128 \n" \
    "buffer_load_dwordx4 v[20:23], v37, s[40:43], s56 offen offset:128 \n" \
    "buffer_load_dwordx4 v[32:35], v38, s[40:43], s56 offen offset:128 \n" \
    "v_mov_b32 v56, 0xbf80bf80\n" \
    "v_mov_b32 v57, 0xbf80bf80\n" \
    "v_mov_b32 v58, 0xbf80bf80\n" \
    "v_mov_b32 v59, 0xbf80bf80\n" \
    "v_lshrrev_b32 v246, 2, v241\n" \
    "v_cmp_eq_u32 vcc, v246, v242\n" \
    "v_and_b32 v246, 1, v241\n" \
    "v_lshlrev_b32 v246, 4, v246\n" \
    "s_movk_i32 s60, 0x42c0\n" \
    "v_lshlrev_b32 v246, v246, s60\n" \
    "v_cndmask_b32 v246, 0, v246, vcc\n" \
    "s_mov_b32 vcc_lo, 0x33333333\n" \
    "s_mov_b32 vcc_hi, 0x33333333\n" \
    "v_cndmask_b32 v62, 0, v246, vcc\n" \
    "v_sub_u32 v63, v246, v62\n" \
    "v_mov_b32 v60, 0\n" \
    "v_mov_b32 v61, 0\n" \
    "v_mov_b32 v64, 0\n" \
    "v_mov_b32 v65, 0\n" \
    "s_waitcnt vmcnt(9)\n" \
    "v_mfma_f32_16x16x32_bf16 v[68:71], v[140:143], v[56:59], 0\n" \
    "v_mfma_f32_16x16x32_bf16 v[72:75], v[152:155], v[56:59], 0\n" \
    "v_mfma_f32_16x16x32_bf16 v[76:79], v[164:167], v[56:59], 0\n" \
    "v_mfma_f32_16x16x32_bf16 v[80:83], v[176:179], v[56:59], 0\n" \
    "v_mfma_f32_16x16x32_bf16 v[84:87], v[188:191], v[56:59], 0\n" \
    "v_mfma_f32_16x16x32_bf16 v[88:91], v[200:203], v[56:59], 0\n" \
    "v_mfma_f32_16x16x32_bf16 v[68:71], v[144:147], v[56:59], v[68:71]\n" \
    "v_mfma_f32_16x16x32_bf16 v[72:75], v[156:159], v[56:59], v[72:75]\n" \
    "v_mfma_f32_16x16x32_bf16 v[76:79], v[168:171], v[56:59], v[76:79]\n" \
    "v_mfma_f32_16x16x32_bf16 v[80:83], v[180:183], v[56:59], v[80:83]\n" \
    "v_mfma_f32_16x16x32_bf16 v[84:87], v[192:195], v[56:59], v[84:87]\n" \
    "v_mfma_f32_16x16x32_bf16 v[88:91], v[204:207], v[56:59], v[88:91]\n" \
    "v_mfma_f32_16x16x32_bf16 v[68:71], v[148:151], v[56:59], v[68:71]\n" \
    "v_mfma_f32_16x16x32_bf16 v[72:75], v[160:163], v[56:59], v[72:75]\n" \
    "v_mfma_f32_16x16x32_bf16 v[76:79], v[172:175], v[56:59], v[76:79]\n" \
    "v_mfma_f32_16x16x32_bf16 v[80:83], v[184:187], v[56:59], v[80:83]\n" \
    "v_mfma_f32_16x16x32_bf16 v[84:87], v[196:199], v[56:59], v[84:87]\n" \
    "v_mfma_f32_16x16x32_bf16 v[88:91], v[208:211], v[56:59], v[88:91]\n" \
    "v_mfma_f32_16x16x32_bf16 v[212:215], v[140:143], v[62:65], v[68:71]\n" \
    "v_mfma_f32_16x16x32_bf16 v[216:219], v[152:155], v[62:65], v[72:75]\n" \
    "v_mfma_f32_16x16x32_bf16 v[220:223], v[140:143], v[60:63], v[68:71]\n" \
    "v_mfma_f32_16x16x32_bf16 v[224:227], v[152:155], v[60:63], v[72:75]\n" \
    "v_mfma_f32_16x16x32_bf16 v[228:231], v[144:147], v[62:65], v[68:71]\n" \
    "v_mfma_f32_16x16x32_bf16 v[232:235], v[156:159], v[62:65], v[72:75]\n" \
    "s_nop 2\n" \
    "v_cvt_pk_bf16_f32 v212, v212, v213\n" \
    "v_cvt_pk_bf16_f32 v213, v214, v215\n" \
    "v_cvt_pk_bf16_f32 v214, v216, v217\n" \
    "v_cvt_pk_bf16_f32 v215, v218, v219\n" \
    "v_mfma_f32_16x16x32_bf16 v[216:219], v[156:159], v[60:63], v[72:75]\n" \
    "ds_write_b128 v40, v[212:215]\n" \
    "v_mfma_f32_16x16x32_bf16 v[212:215], v[144:147], v[60:63], v[68:71]\n" \
    "v_cvt_pk_bf16_f32 v220, v220, v221\n" \
    "v_cvt_pk_bf16_f32 v221, v222, v223\n" \
    "v_cvt_pk_bf16_f32 v222, v224, v225\n" \
    "v_cvt_pk_bf16_f32 v223, v226, v227\n" \
    "v_mfma_f32_16x16x32_bf16 v[224:227], v[160:163], v[62:65], v[72:75]\n" \
    "ds_write_b128 v40, v[220:223] offset:3072\n" \
    "v_mfma_f32_16x16x32_bf16 v[220:223], v[148:151], v[62:65], v[68:71]\n" \
    "v_cvt_pk_bf16_f32 v228, v228, v229\n" \
    "v_cvt_pk_bf16_f32 v229, v230, v231\n" \
    "v_cvt_pk_bf16_f32 v230, v232, v233\n" \
    "v_cvt_pk_bf16_f32 v231, v234, v235\n" \
    "v_mfma_f32_16x16x32_bf16 v[232:235], v[160:163], v[60:63], v[72:75]\n" \
    "ds_write_b128 v40, v[228:231] offset:6144\n" \
    "v_mfma_f32_16x16x32_bf16 v[228:231], v[148:151], v[60:63], v[68:71]\n" \
    "v_cvt_pk_bf16_f32 v212, v212, v213\n" \
    "v_cvt_pk_bf16_f32 v213, v214, v215\n" \
    "v_cvt_pk_bf16_f32 v214, v216, v217\n" \
    "v_cvt_pk_bf16_f32 v215, v218, v219\n" \
    "v_mfma_f32_16x16x32_bf16 v[216:219], v[176:179], v[62:65], v[80:83]\n" \
    "ds_write_b128 v40, v[212:215] offset:9216\n" \
    "v_mfma_f32_16x16x32_bf16 v[212:215], v[164:167], v[62:65], v[76:79]\n" \
    "v_cvt_pk_bf16_f32 v220, v220, v221\n" \
    "v_cvt_pk_bf16_f32 v221, v222, v223\n" \
    "v_cvt_pk_bf16_f32 v222, v224, v225\n" \
    "v_cvt_pk_bf16_f32 v223, v226, v227\n" \
    "v_mfma_f32_16x16x32_bf16 v[224:227], v[176:179], v[60:63], v[80:83]\n" \
    "ds_write_b128 v40, v[220:223] offset:12288\n" \
    "v_mfma_f32_16x16x32_bf16 v[220:223], v[164:167], v[60:63], v[76:79]\n" \
    "v_cvt_pk_bf16_f32 v228, v228, v229\n" \
    "v_cvt_pk_bf16_f32 v229, v230, v231\n" \
    "v_cvt_pk_bf16_f32 v230, v232, v233\n" \
    "v_cvt_pk_bf16_f32 v231, v234, v235\n" \
    "v_mfma_f32_16x16x32_bf16 v[232:235], v[180:183], v[62:65], v[80:83]\n" \
    "ds_write_b128 v40, v[228:231] offset:15360\n" \
    "v_mfma_f32_16x16x32_bf16 v[228:231], v[168:171], v[62:65], v[76:79]\n" \
    "v_cvt_pk_bf16_f32 v212, v212, v213\n" \
    "v_cvt_pk_bf16_f32 v213, v214, v215\n" \
    "v_cvt_pk_bf16_f32 v214, v216, v217\n" \
    "v_cvt_pk_bf16_f32 v215, v218, v219\n" \
    "v_mfma_f32_16x16x32_bf16 v[216:219], v[180:183], v[60:63], v[80:83]\n" \
    "ds_write_b128 v40, v[212:215] offset:1024\n" \
    "v_mfma_f32_16x16x32_bf16 v[212:215], v[168:171], v[60:63], v[76:79]\n" \
    "v_cvt_pk_bf16_f32 v220, v220, v221\n" \
    "v_cvt_pk_bf16_f32 v221, v222, v223\n" \
    "v_cvt_pk_bf16_f32 v222, v224, v225\n" \
    "v_cvt_pk_bf16_f32 v223, v226, v227\n" \
    "v_mfma_f32_16x16x32_bf16 v[224:227], v[184:187], v[62:65], v[80:83]\n" \
    "ds_write_b128 v40, v[220:223] offset:4096\n" \
    "v_mfma_f32_16x16x32_bf16 v[220:223], v[172:175], v[62:65], v[76:79]\n" \
    "v_cvt_pk_bf16_f32 v228, v228, v229\n" \
    "v_cvt_pk_bf16_f32 v229, v230, v231\n" \
    "v_cvt_pk_bf16_f32 v230, v232, v233\n" \
    "v_cvt_pk_bf16_f32 v231, v234, v235\n" \
    "v_mfma_f32_16x16x32_bf16 v[232:235], v[184:187], v[60:63], v[80:83]\n" \
    "ds_write_b128 v40, v[228:231] offset:7168\n" \
    "v_mfma_f32_16x16x32_bf16 v[228:231], v[172:175], v[60:63], v[76:79]\n" \
    "v_cvt_pk_bf16_f32 v212, v212, v213\n" \
    "v_cvt_pk_bf16_f32 v213, v214, v215\n" \
    "v_cvt_pk_bf16_f32 v214, v216, v217\n" \
    "v_cvt_pk_bf16_f32 v215, v218, v219\n" \
    "v_mfma_f32_16x16x32_bf16 v[216:219], v[200:203], v[62:65], v[88:91]\n" \
    "ds_write_b128 v40, v[212:215] offset:10240\n" \
    "v_mfma_f32_16x16x32_bf16 v[212:215], v[188:191], v[62:65], v[84:87]\n" \
    "v_cvt_pk_bf16_f32 v220, v220, v221\n" \
    "v_cvt_pk_bf16_f32 v221, v222, v223\n" \
    "v_cvt_pk_bf16_f32 v222, v224, v225\n" \
    "v_cvt_pk_bf16_f32 v223, v226, v227\n" \
    "v_mfma_f32_16x16x32_bf16 v[224:227], v[200:203], v[60:63], v[88:91]\n" \
    "ds_write_b128 v40, v[220:223] offset:13312\n" \
    "v_mfma_f32_16x16x32_bf16 v[220:223], v[188:191], v[60:63], v[84:87]\n" \
    "v_cvt_pk_bf16_f32 v228, v228, v229\n" \
    "v_cvt_pk_bf16_f32 v229, v230, v231\n" \
    "v_cvt_pk_bf16_f32 v230, v232, v233\n" \
    "v_cvt_pk_bf16_f32 v231, v234, v235\n" \
    "v_mfma_f32_16x16x32_bf16 v[232:235], v[204:207], v[62:65], v[88:91]\n" \
    "ds_write_b128 v40, v[228:231] offset:16384\n" \
    "v_mfma_f32_16x16x32_bf16 v[228:231], v[192:195], v[62:65], v[84:87]\n" \
    "v_cvt_pk_bf16_f32 v212, v212, v213\n" \
    "v_cvt_pk_bf16_f32 v213, v214, v215\n" \
    "v_cvt_pk_bf16_f32 v214, v216, v217\n" \
    "v_cvt_pk_bf16_f32 v215, v218, v219\n" \
    "v_mfma_f32_16x16x32_bf16 v[216:219], v[204:207], v[60:63], v[88:91]\n" \
    "ds_write_b128 v40, v[212:215] offset:2048\n" \
    "v_mfma_f32_16x16x32_bf16 v[212:215], v[192:195], v[60:63], v[84:87]\n" \
    "v_cvt_pk_bf16_f32 v220, v220, v221\n" \
    "v_cvt_pk_bf16_f32 v221, v222, v223\n" \
    "v_cvt_pk_bf16_f32 v222, v224, v225\n" \
    "v_cvt_pk_bf16_f32 v223, v226, v227\n" \
    "v_mfma_f32_16x16x32_bf16 v[224:227], v[208:211], v[62:65], v[88:91]\n" \
    "v_cvt_pk_bf16_f32 v228, v228, v229\n" \
    "v_cvt_pk_bf16_f32 v229, v230, v231\n" \
    "v_cvt_pk_bf16_f32 v230, v232, v233\n" \
    "v_cvt_pk_bf16_f32 v231, v234, v235\n" \
    "v_mfma_f32_16x16x32_bf16 v[232:235], v[208:211], v[60:63], v[88:91]\n" \
    "v_cvt_pk_bf16_f32 v212, v212, v213\n" \
    "v_cvt_pk_bf16_f32 v213, v214, v215\n" \
    "v_cvt_pk_bf16_f32 v214, v216, v217\n" \
    "v_cvt_pk_bf16_f32 v215, v218, v219\n" \
    "ds_write_b128 v40, v[220:223] offset:5120\n" \
    "v_mfma_f32_16x16x32_bf16 v[220:223], v[196:199], v[62:65], v[84:87]\n" \
    "ds_write_b128 v40, v[228:231] offset:8192\n" \
    "v_mfma_f32_16x16x32_bf16 v[228:231], v[196:199], v[60:63], v[84:87]\n" \
    "s_nop 5\n" \
    "v_cvt_pk_bf16_f32 v220, v220, v221\n" \
    "v_cvt_pk_bf16_f32 v221, v222, v223\n" \
    "v_cvt_pk_bf16_f32 v222, v224, v225\n" \
    "v_cvt_pk_bf16_f32 v223, v226, v227\n" \
    "v_cvt_pk_bf16_f32 v228, v228, v229\n" \
    "v_cvt_pk_bf16_f32 v229, v230, v231\n" \
    "v_cvt_pk_bf16_f32 v230, v232, v233\n" \
    "v_cvt_pk_bf16_f32 v231, v234, v235\n" \
    "ds_write_b128 v40, v[212:215] offset:11264\n" \
    "ds_write_b128 v40, v[220:223] offset:14336\n" \
    "ds_write_b128 v40, v[228:231] offset:17408\n" \
    "v_mov_b32 v246, 148744\n" \
    "v_mov_b32 v247, 1\n" \
    "s_mov_b64 exec, 1\n" \
    "ds_add_u32 v246, v247\n" \
    "s_mov_b64 exec, -1\n" \
    "s_nop 0\n" \
    "v_mov_b32 v246, 148736\n" \
    "s_branch Lwde%=\n" \
    "Lwd%=:\n" \
    "s_sleep 1\n" \
    "Lwde%=:\n" \
    "ds_read_b32 v247, v246\n" \
    "s_waitcnt lgkmcnt(0)\n" \
    "v_cmp_gt_u32 vcc, 4, v247\n" \
    "s_nop 1\n" \
    "s_cbranch_vccnz Lwd%=\n" \
    "s_branch LproF%=\n" \
    "LproX%=:\n" \
    "v_mov_b32 v246, 148736\n" \
    "s_branch Lwbe%=\n" \
    "Lwb%=:\n" \
    "s_sleep 1\n" \
    "Lwbe%=:\n" \
    "ds_read_b32 v247, v246\n" \
    "s_waitcnt lgkmcnt(0)\n" \
    "v_cmp_gt_u32 vcc, 4, v247\n" \
    "s_nop 1\n" \
    "s_cbranch_vccnz Lwb%=\n" \
    "s_mul_i32 s60, s98, 18720\n" \
    "v_add_u32 v246, s60, v39\n" \
    "ds_read_b128 v[56:59], v246\n" \
    "ds_read_b128 v[60:63], v246 offset:1040\n" \
    "ds_read_b128 v[64:67], v246 offset:2080\n" \
    "ds_read_b128 v[68:71], v246 offset:3120\n" \
    "ds_read_b128 v[72:75], v246 offset:4160\n" \
    "ds_read_b128 v[76:79], v246 offset:5200\n" \
    "ds_read_b128 v[80:83], v246 offset:6240\n" \
    "ds_read_b128 v[84:87], v246 offset:7280\n" \
    "ds_read_b128 v[88:91], v246 offset:8320\n" \
    "ds_read_b128 v[164:167], v246 offset:9360\n" \
    "ds_read_b128 v[168:171], v246 offset:10400\n" \
    "ds_read_b128 v[172:175], v246 offset:11440\n" \
    "ds_read_b128 v[176:179], v246 offset:12480\n" \
    "ds_read_b128 v[180:183], v246 offset:13520\n" \
    "ds_read_b128 v[184:187], v246 offset:14560\n" \
    "ds_read_b128 v[188:191], v246 offset:15600\n" \
    "ds_read_b128 v[192:195], v246 offset:16640\n" \
    "ds_read_b128 v[196:199], v246 offset:17680\n" \
    "s_waitcnt vmcnt(0) lgkmcnt(0)\n" \
    "v_mfma_f32_16x16x32_bf16 v[140:143], v[56:59], v[228:231], 0\n" \
    "v_mfma_f32_16x16x32_bf16 v[144:147], v[68:71], v[228:231], 0\n" \
    "v_mfma_f32_16x16x32_bf16 v[148:151], v[80:83], v[228:231], 0\n" \
    "v_mfma_f32_16x16x32_bf16 v[152:155], v[164:167], v[228:231], 0\n" \
    "v_mfma_f32_16x16x32_bf16 v[156:159], v[176:179], v[228:231], 0\n" \
    "v_mfma_f32_16x16x32_bf16 v[160:163], v[188:191], v[228:231], 0\n" \
    "v_mfma_f32_16x16x32_bf16 v[140:143], v[60:63], v[232:235], v[140:143]\n" \
    "v_mfma_f32_16x16x32_bf16 v[144:147], v[72:75], v[232:235], v[144:147]\n" \
    "v_mfma_f32_16x16x32_bf16 v[148:151], v[84:87], v[232:235], v[148:151]\n" \
    "v_mfma_f32_16x16x32_bf16 v[152:155], v[168:171], v[232:235], v[152:155]\n" \
    "v_mfma_f32_16x16x32_bf16 v[156:159], v[180:183], v[232:235], v[156:159]\n" \
    "v_mfma_f32_16x16x32_bf16 v[160:163], v[192:195], v[232:235], v[160:163]\n" \
    "v_mfma_f32_16x16x32_bf16 v[140:143], v[64:67], v[236:239], v[140:143]\n" \
    "v_mfma_f32_16x16x32_bf16 v[144:147], v[76:79], v[236:239], v[144:147]\n" \
    "v_mfma_f32_16x16x32_bf16 v[148:151], v[88:91], v[236:239], v[148:151]\n" \
    "v_mfma_f32_16x16x32_bf16 v[152:155], v[172:175], v[236:239], v[152:155]\n" \
    "v_mfma_f32_16x16x32_bf16 v[156:159], v[184:187], v[236:239], v[156:159]\n" \
    "v_mfma_f32_16x16x32_bf16 v[160:163], v[196:199], v[236:239], v[160:163]\n" \
    "buffer_load_dwordx4 v[0:3], v36, s[40:43], s56 offen \n" \
    "buffer_load_dwordx4 v[12:15], v37, s[40:43], s56 offen \n" \
    "buffer_load_dwordx4 v[24:27], v38, s[40:43], s56 offen \n" \
    "buffer_load_dwordx4 v[4:7], v36, s[40:43], s56 offen offset:64 \n" \
    "buffer_load_dwordx4 v[16:19], v37, s[40:43], s56 offen offset:64 \n" \
    "buffer_load_dwordx4 v[28:31], v38, s[40:43], s56 offen offset:64 \n" \
    "buffer_load_dwordx4 v[8:11], v36, s[40:43], s56 offen offset:128 \n" \
    "buffer_load_dwordx4 v[20:23], v37, s[40:43], s56 offen offset:128 \n" \
    "buffer_load_dwordx4 v[32:35], v38, s[40:43], s56 offen offset:128 \n" \
    "v_mul_u32_u24 v246, 2064, v241\n" \
    "s_cmp_eq_u32 s98, 2\n" \
    "s_cbranch_scc1 LproV%=\n" \
    "s_lshl_b32 s60, s98, 8\n" \
    "s_add_u32 s60, s60, 115712\n" \
    "v_lshl_add_u32 v246, v242, 4, v246\n" \
    "v_add_u32 v246, s60, v246\n" \
    "s_cmp_eq_u32 s98, 0\n" \
    "s_cselect_b32 s60, 1.0, s36\n" \
    "s_cselect_b32 s61, 1.0, s37\n" \
    "s_cselect_b32 s62, 1.0, s38\n" \
    "s_cselect_b32 s63, 1.0, s39\n" \
    "v_fmaak_f32 v60, v140, v140, 0x2a8cbccc\n" \
    "v_fmac_f32 v60, v141, v141\n" \
    "v_fmac_f32 v60, v142, v142\n" \
    "v_fmac_f32 v60, v143, v143\n" \
    "v_fmac_f32 v60, v144, v144\n" \
    "v_fmac_f32 v60, v145, v145\n" \
    "v_fmaak_f32 v61, v146, v146, 0x2a8cbccc\n" \
    "v_fmac_f32 v61, v147, v147\n" \
    "v_fmac_f32 v61, v148, v148\n" \
    "v_fmac_f32 v61, v149, v149\n" \
    "v_fmac_f32 v61, v150, v150\n" \
    "v_fmac_f32 v61, v151, v151\n" \
    "v_fmaak_f32 v62, v152, v152, 0x2a8cbccc\n" \
    "v_fmac_f32 v62, v153, v153\n" \
    "v_fmac_f32 v62, v154, v154\n" \
    "v_fmac_f32 v62, v155, v155\n" \
    "v_fmac_f32 v62, v156, v156\n" \
    "v_fmac_f32 v62, v157, v157\n" \
    "v_fmaak_f32 v63, v158, v158, 0x2a8cbccc\n" \
    "v_fmac_f32 v63, v159, v159\n" \
    "v_fmac_f32 v63, v160, v160\n" \
    "v_fmac_f32 v63, v161, v161\n" \
    "v_fmac_f32 v63, v162, v162\n" \
    "v_fmac_f32 v63, v163, v163\n" \
    "v_mov_b32 v64, v60\n" \
    "v_mov_b32 v65, v61\n" \
    "v_mov_b32 v66, v62\n" \
    "v_mov_b32 v67, v63\n" \
    "v_permlane32_swap_b32 v60, v64\n" \
    "v_permlane32_swap_b32 v61, v65\n" \
    "v_permlane32_swap_b32 v62, v66\n" \
    "v_permlane32_swap_b32 v63, v67\n" \
    "v_add_f32 v60, v60, v64\n" \
    "v_add_f32 v61, v61, v65\n" \
    "v_add_f32 v62, v62, v66\n" \
    "v_add_f32 v63, v63, v67\n" \
    "v_mov_b32 v64, v60\n" \
    "v_mov_b32 v65, v61\n" \
    "v_mov_b32 v66, v62\n" \
    "v_mov_b32 v67, v63\n" \
    "v_permlane16_swap_b32 v60, v64\n" \
    "v_permlane16_swap_b32 v61, v65\n" \
    "v_permlane16_swap_b32 v62, v66\n" \
    "v_permlane16_swap_b32 v63, v67\n" \
    "v_add_f32 v60, v60, v64\n" \
    "v_add_f32 v61, v61, v65\n" \
    "v_add_f32 v62, v62, v66\n" \
    "v_add_f32 v63, v63, v67\n" \
    "v_rsq_f32 v60, v60\n" \
    "v_rsq_f32 v61, v61\n" \
    "v_rsq_f32 v62, v62\n" \
    "v_rsq_f32 v63, v63\n" \
    "v_mul_f32 v60, s60, v60\n" \
    "v_mul_f32 v61, s61, v61\n" \
    "v_mul_f32 v62, s62, v62\n" \
    "v_mul_f32 v63, s63, v63\n" \
    "v_mul_f32 v140, v140, v60\n" \
    "v_mul_f32 v141, v141, v60\n" \
    "v_mul_f32 v142, v142, v60\n" \
    "v_mul_f32 v143, v143, v60\n" \
    "v_mul_f32 v144, v144, v60\n" \
    "v_mul_f32 v145, v145, v60\n" \
    "v_cvt_pk_f16_f32 v68, v140, v141\n" \
    "v_cvt_pk_f16_f32 v69, v142, v143\n" \
    "v_cvt_pk_f16_f32 v70, v144, v145\n" \
    "v_mov_b32 v71, 0\n" \
    "ds_write_b128 v246, v[68:71]\n" \
    "v_mul_f32 v146, v146, v61\n" \
    "v_mul_f32 v147, v147, v61\n" \
    "v_mul_f32 v148, v148, v61\n" \
    "v_mul_f32 v149, v149, v61\n" \
    "v_mul_f32 v150, v150, v61\n" \
    "v_mul_f32 v151, v151, v61\n" \
    "v_cvt_pk_f16_f32 v73, v146, v147\n" \
    "v_cvt_pk_f16_f32 v74, v148, v149\n" \
    "v_cvt_pk_f16_f32 v75, v150, v151\n" \
    "v_mov_b32 v72, 0\n" \
    "ds_write_b128 v246, v[72:75] offset:64\n" \
    "v_mul_f32 v152, v152, v62\n" \
    "v_mul_f32 v153, v153, v62\n" \
    "v_mul_f32 v154, v154, v62\n" \
    "v_mul_f32 v155, v155, v62\n" \
    "v_mul_f32 v156, v156, v62\n" \
    "v_mul_f32 v157, v157, v62\n" \
    "v_cvt_pk_f16_f32 v76, v152, v153\n" \
    "v_cvt_pk_f16_f32 v77, v154, v155\n" \
    "v_cvt_pk_f16_f32 v78, v156, v157\n" \
    "v_mov_b32 v79, 0\n" \
    "ds_write_b128 v246, v[76:79] offset:128\n" \
    "v_mul_f32 v158, v158, v63\n" \
    "v_mul_f32 v159, v159, v63\n" \
    "v_mul_f32 v160, v160, v63\n" \
    "v_mul_f32 v161, v161, v63\n" \
    "v_mul_f32 v162, v162, v63\n" \
    "v_mul_f32 v163, v163, v63\n" \
    "v_cvt_pk_f16_f32 v81, v158, v159\n" \
    "v_cvt_pk_f16_f32 v82, v160, v161\n" \
    "v_cvt_pk_f16_f32 v83, v162, v163\n" \
    "v_mov_b32 v80, 0\n" \
    "ds_write_b128 v246, v[80:83] offset:192\n" \
    "s_branch LproE%=\n" \
    "LproV%=:\n" \
    "s_nop 7\n" \
    "v_lshl_add_u32 v246, v242, 5, v246\n" \
    "v_add_u32 v246, 116736, v246\n" \
    "v_mov_b32 v56, 0x3f80\n" \
    "v_mov_b32 v57, 0\n" \
    "v_mov_b32 v58, 0x3f80\n" \
    "v_mov_b32 v59, 0\n" \
    "v_cvt_pk_bf16_f32 v164, v140, 0\n" \
    "v_mov_b32 v165, 0\n" \
    "v_cvt_pk_bf16_f32 v166, v141, 0\n" \
    "v_mov_b32 v167, 0\n" \
    "v_cvt_pk_bf16_f32 v168, v142, 0\n" \
    "v_mov_b32 v169, 0\n" \
    "v_cvt_pk_bf16_f32 v170, v143, 0\n" \
    "v_mov_b32 v171, 0\n" \
    "v_cvt_pk_bf16_f32 v172, v144, 0\n" \
    "v_mov_b32 v173, 0\n" \
    "v_cvt_pk_bf16_f32 v174, v145, 0\n" \
    "v_mov_b32 v175, 0\n" \
    "v_cvt_pk_bf16_f32 v176, v146, 0\n" \
    "v_mov_b32 v177, 0\n" \
    "v_cvt_pk_bf16_f32 v178, v147, 0\n" \
    "v_mov_b32 v179, 0\n" \
    "v_cvt_pk_bf16_f32 v180, v148, 0\n" \
    "v_mov_b32 v181, 0\n" \
    "v_cvt_pk_bf16_f32 v182, v149, 0\n" \
    "v_mov_b32 v183, 0\n" \
    "v_cvt_pk_bf16_f32 v184, v150, 0\n" \
    "v_mov_b32 v185, 0\n" \
    "v_cvt_pk_bf16_f32 v186, v151, 0\n" \
    "v_mov_b32 v187, 0\n" \
    "v_cvt_pk_bf16_f32 v188, v152, 0\n" \
    "v_mov_b32 v189, 0\n" \
    "v_cvt_pk_bf16_f32 v190, v153, 0\n" \
    "v_mov_b32 v191, 0\n" \
    "v_cvt_pk_bf16_f32 v192, v154, 0\n" \
    "v_mov_b32 v193, 0\n" \
    "v_cvt_pk_bf16_f32 v194, v155, 0\n" \
    "v_mov_b32 v195, 0\n" \
    "v_cvt_pk_bf16_f32 v196, v156, 0\n" \
    "v_mov_b32 v197, 0\n" \
    "v_cvt_pk_bf16_f32 v198, v157, 0\n" \
    "v_mov_b32 v199, 0\n" \
    "v_cvt_pk_bf16_f32 v200, v158, 0\n" \
    "v_mov_b32 v201, 0\n" \
    "v_cvt_pk_bf16_f32 v202, v159, 0\n" \
    "v_mov_b32 v203, 0\n" \
    "v_cvt_pk_bf16_f32 v204, v160, 0\n" \
    "v_mov_b32 v205, 0\n" \
    "v_cvt_pk_bf16_f32 v206, v161, 0\n" \
    "v_mov_b32 v207, 0\n" \
    "v_cvt_pk_bf16_f32 v208, v162, 0\n" \
    "v_mov_b32 v209, 0\n" \
    "v_cvt_pk_bf16_f32 v210, v163, 0\n" \
    "v_mov_b32 v211, 0\n" \
    "ds_write_b128 v246, v[164:167]\n" \
    "ds_write_b128 v246, v[168:171] offset:16\n" \
    "ds_write_b128 v246, v[172:175] offset:128\n" \
    "ds_write_b128 v246, v[56:59] offset:144\n" \
    "ds_write_b128 v246, v[56:59] offset:384\n" \
    "ds_write_b128 v246, v[176:179] offset:400\n" \
    "ds_write_b128 v246, v[180:183] offset:256\n" \
    "ds_write_b128 v246, v[184:187] offset:272\n" \
    "ds_write_b128 v246, v[188:191] offset:512\n" \
    "ds_write_b128 v246, v[192:195] offset:528\n" \
    "ds_write_b128 v246, v[196:199] offset:640\n" \
    "ds_write_b128 v246, v[56:59] offset:656\n" \
    "ds_write_b128 v246, v[56:59] offset:896\n" \
    "ds_write_b128 v246, v[200:203] offset:912\n" \
    "ds_write_b128 v246, v[204:207] offset:768\n" \
    "ds_write_b128 v246, v[208:211] offset:784\n" \
    "LproE%=:\n" \
    "s_nop 1\n" \
    "v_mov_b32 v246, 148740\n" \
    "v_mov_b32 v247, 1\n" \
    "s_mov_b64 exec, 1\n" \
    "ds_add_u32 v246, v247\n" \
    "s_mov_b64 exec, -1\n" \
    "LproF%=:\n" \
    "s_setprio 1\n" \
    "s_mov_b32 s44, s92\n" \
    "s_and_b32 s45, s93, 0xffff\n" \
    "s_mov_b32 s46, 14002632\n" \
    "s_mov_b32 s47, 0x20000\n" \
    "s_min_u32 s66, s52, 2808\n" \
    "s_mul_i32 s60, s66, 19785\n" \
    "s_lshr_b32 s60, s60, 20\n" \
    "s_add_u32 s61, s66, s60\n" \
    "s_mul_i32 s62, s61, 4802\n" \
    "s_and_b32 s63, s62, 15\n" \
    "s_sub_u32 s62, s62, s63\n" \
    "s_add_u32 s64, s63, 690\n" \
    "s_add_u32 s65, s62, 4096\n" \
    "buffer_load_dwordx4 v[188:191], v39, s[44:47], s62 offen offset:1024\n" \
    "buffer_load_dwordx4 v[192:195], v39, s[44:47], s62 offen offset:2048\n" \
    "buffer_load_dwordx4 v[196:199], v39, s[44:47], s62 offen offset:3072\n" \
    "v_max_u32 v200, s63, v39\n" \
    "v_min_u32 v252, s64, v39\n" \
    "buffer_load_dwordx4 v[200:203], v200, s[44:47], s62 offen\n" \
    "buffer_load_dwordx4 v[252:255], v252, s[44:47], s65 offen\n" \
    "s_mov_b32 s76, 0xffff\n" \
    "s_mov_b32 s77, 0x0\n" \
    "s_mov_b32 s78, 0xffff0000\n" \
    "s_mov_b32 s79, 0x0\n" \
    "s_mov_b32 s80, 0x0\n" \
    "s_mov_b32 s81, 0xffff\n" \
    "s_mov_b32 s82, 0x0\n" \
    "s_mov_b32 s83, 0xffff0000\n" \
    "s_waitcnt vmcnt(5)\n" \
    "s_branch Lgo%=\n" \
    "LproA%=:\n" \
    "s_cmp_lt_u32 s74, 156\n" \
    "s_cselect_b32 s60, 1, 0\n" \
    "s_mul_i32 s61, s74, 10\n" \
    "s_mul_i32 s62, s74, 12\n" \
    "s_sub_u32 s62, s62, 312\n" \
    "s_max_i32 s71, s61, s62\n" \
    "s_sub_u32 s53, 4, s60\n" \
    "s_sub_u32 s34, 12, s60\n" \
    "s_lshr_b32 s61, s75, 2\n" \
    "s_lshl_b32 s62, s61, 2\n" \
    "s_add_u32 s35, s75, s62\n" \
    "s_lshl_b32 s62, s60, s61\n" \
    "s_add_u32 s52, s71, s35\n" \
    "s_sub_u32 s52, s52, s62\n" \
    "s_sub_u32 s54, 2, s61\n" \
    "s_lshl_b32 s62, s60, 3\n" \
    "s_or_b32 s62, s62, s75\n" \
    "s_add_u32 s63, s74, 2760\n" \
    "s_cmp_eq_u32 s62, 8\n" \
    "s_cselect_b32 s54, 1, s54\n" \
    "s_cselect_b32 s52, s63, s52\n" \
    "s_min_u32 s63, s52, 2808\n" \
    "s_mul_i32 s61, s63, 19785\n" \
    "s_lshr_b32 s61, s61, 20\n" \
    "s_mul_i32 s61, s61, 436800\n" \
    "s_mul_i32 s56, s63, 1344\n" \
    "s_add_u32 s56, s56, s61\n" \
    "s_add_u32 s56, s56, 218304\n" \
    "s_mov_b32 s96, 71232\n" \
    "s_movk_i32 s97, 192\n" \
    "v_lshlrev_b32 v247, 4, v242\n" \
    "s_mov_b32 s99, s60\n" \
    "v_add_u32 v246, 0, v241\n" \
    "v_mul_u32_u24 v248, 37, v246\n" \
    "v_lshrrev_b32 v248, 8, v248\n" \
    "v_mad_u32_u24 v246, v246, s97, v247\n" \
    "v_mad_u32_u24 v36, v248, s96, v246\n" \
    "v_add_u32 v246, 16, v241\n" \
    "v_mul_u32_u24 v248, 37, v246\n" \
    "v_lshrrev_b32 v248, 8, v248\n" \
    "v_mad_u32_u24 v246, v246, s97, v247\n" \
    "v_mad_u32_u24 v37, v248, s96, v246\n" \
    "v_add_u32 v246, 32, v241\n" \
    "v_mul_u32_u24 v248, 37, v246\n" \
    "v_lshrrev_b32 v248, 8, v248\n" \
    "v_mad_u32_u24 v246, v246, s97, v247\n" \
    "v_mad_u32_u24 v38, v248, s96, v246\n" \
    "s_movk_i32 s59, 4608\n" \
    "s_waitcnt lgkmcnt(0)\n" \
    "s_mov_b32 s44, s78\n" \
    "s_and_b32 s45, s79, 0xffff\n" \
    "s_mov_b32 s46, 55296\n" \
    "s_mov_b32 s47, 0x20000\n" \
    "s_mov_b32 s40, s76\n" \
    "s_and_b32 s41, s77, 0xffff\n" \
    "s_mov_b32 s42, 27433728\n" \
    "s_mov_b32 s43, 0x20000\n" \
    "buffer_load_dwordx4 v[140:143], v244, s[44:47], 0 offen\n" \
    "buffer_load_dwordx4 v[144:147], v244, s[44:47], 0 offen offset:576\n" \
    "buffer_load_dwordx4 v[148:151], v244, s[44:47], 0 offen offset:1152\n" \
    "buffer_load_dwordx4 v[152:155], v244, s[44:47], 0 offen offset:1728\n" \
    "buffer_load_dwordx4 v[156:159], v244, s[44:47], 0 offen offset:2304\n" \
    "buffer_load_dwordx4 v[160:163], v244, s[44:47], 0 offen offset:2880\n" \
    "buffer_load_dwordx4 v[164:167], v244, s[44:47], 0 offen offset:3456\n" \
    "s_mov_b32 s64, s82\n" \
    "s_and_b32 s65, s83, 0xffff\n" \
    "s_mov_b32 s66, 12\n" \
    "s_mov_b32 s67, 0x20000\n" \
    "buffer_load_ushort v249, v249, s[64:67], 0 offen\n" \
    "v_mul_u32_u24 v246, 36, v245\n" \
    "v_sub_u32 v246, v243, v246\n" \
    "v_add_u32 v247, 12, v246\n" \
    "v_subrev_u32 v248, 12, v246\n" \
    "v_cmp_gt_u32 vcc, 12, v246\n" \
    "s_nop 1\n" \
    "v_cndmask_b32 v247, v248, v247, vcc\n" \
    "v_cmp_gt_u32 vcc, 24, v246\n" \
    "s_nop 1\n" \
    "v_cndmask_b32 v246, v246, v247, vcc\n" \
    "v_lshrrev_b32 v247, 1, v246\n" \
    "v_mul_u32_u24 v248, 43, v247\n" \
    "v_lshrrev_b32 v248, 7, v248\n" \
    "v_mul_u32_u24 v248, 3, v248\n" \
    "v_sub_u32 v248, v247, v248\n" \
    "v_cmp_eq_u32 vcc, 1, v248\n" \
    "v_mul_u32_u24 v247, 3, v247\n" \
    "v_lshrrev_b32 v248, 1, v245\n" \
    "v_add_u32 v247, v247, v248\n" \
    "v_mul_u32_u24 v247, 1040, v247\n" \
    "v_and_b32 v248, 1, v245\n" \
    "v_lshl_add_u32 v247, v248, 9, v247\n" \
    "v_and_b32 v246, 1, v246\n" \
    "v_cndmask_b32 v248, 7, 5, vcc\n" \
    "v_lshl_add_u32 v247, v246, v248, v247\n" \
    "v_cndmask_b32 v248, 32, 64, vcc\n" \
    "v_cmp_gt_u32 vcc, 6, v245\n" \
    "v_mov_b32 v246, 196608\n" \
    "s_mov_b32 s60, 0x05040100\n" \
    "s_mov_b32 s61, 0x07060302\n" \
    "v_cndmask_b32 v232, v246, v247, vcc\n" \
    "v_add_u32 v233, v232, v248\n" \
    "v_add_u32 v234, v233, v248\n" \
    "v_add_u32 v235, v234, v248\n" \
    "s_setprio 2\n" \
    "s_barrier\n" \
    "v_lshlrev_b32 v39, 4, v240\n" \
    "v_mov_b32 v251, v244\n" \
    "s_mov_b32 s48, s94\n" \
    "buffer_load_dwordx4 v[168:171], v251, s[44:47], 0 offen offset:4032\n" \
    "s_and_b32 s49, s95, 0xffff\n" \
    "s_mov_b32 s50, 27433728\n" \
    "s_mov_b32 s51, 0x20000\n" \
    "buffer_load_dwordx4 v[172:175], v251, s[44:47], s59 offen\n" \
    "v_add_u32 v40, 56192, v39\n" \
    "v_and_b32 v246, 1, v242\n" \
    "v_xor_b32 v246, v246, v240\n" \
    "buffer_load_dwordx4 v[176:179], v251, s[44:47], s59 offen offset:576\n" \
    "v_lshlrev_b32 v246, 4, v246\n" \
    "v_add_u32 v41, 74624, v246\n" \
    "v_lshrrev_b32 v246, 2, v241\n" \
    "buffer_load_dwordx4 v[180:183], v251, s[44:47], s59 offen offset:1152\n" \
    "v_lshlrev_b32 v247, 4, v242\n" \
    "v_lshl_add_u32 v248, v246, 8, v247\n" \
    "v_add_u32 v42, 114688, v248\n" \
    "buffer_load_dwordx4 v[184:187], v251, s[44:47], s59 offen offset:1728\n" \
    "v_lshlrev_b32 v43, 2, v241\n" \
    "s_mul_i32 s60, s35, 2064\n" \
    "s_add_u32 s60, s60, 115712\n" \
    "buffer_load_dwordx4 v[188:191], v251, s[44:47], s59 offen offset:2304\n" \
    "v_lshl_add_u32 v54, v241, 3, s60\n" \
    "v_lshl_add_u32 v247, v241, 4, v247\n" \
    "v_add_u32 v247, s60, v247\n" \
    "buffer_load_dwordx4 v[192:195], v251, s[44:47], s59 offen offset:2880\n" \
    "v_mov_b32 v248, 196608\n" \
    "v_mov_b32 v244, 0x3c003c00\n" \
    "v_cmp_eq_u32 vcc, 0, v241\n" \
    "buffer_load_dwordx4 v[196:199], v251, s[44:47], s59 offen offset:3456\n" \
    "v_cmp_eq_u32 s[60:61], 0, v246\n" \
    "s_nop 0\n" \
    "v_cndmask_b32 v44, v248, v247, vcc\n" \
    "buffer_load_dwordx4 v[200:203], v251, s[44:47], s59 offen offset:4032\n" \
    "v_cndmask_b32 v48, 0, v244, s[60:61]\n" \
    "v_cmp_eq_u32 vcc, 4, v241\n" \
    "v_cmp_eq_u32 s[60:61], 1, v246\n" \
    "buffer_load_dwordx4 v[0:3], v36, s[40:43], s56 offen \n" \
    "s_nop 0\n" \
    "v_cndmask_b32 v45, v248, v247, vcc\n" \
    "v_cndmask_b32 v49, 0, v244, s[60:61]\n" \
    "buffer_load_dwordx4 v[12:15], v37, s[40:43], s56 offen \n" \
    "v_cmp_eq_u32 vcc, 8, v241\n" \
    "v_cmp_eq_u32 s[60:61], 2, v246\n" \
    "s_nop 0\n" \
    "buffer_load_dwordx4 v[24:27], v38, s[40:43], s56 offen \n" \
    "v_cndmask_b32 v46, v248, v247, vcc\n" \
    "v_cndmask_b32 v50, 0, v244, s[60:61]\n" \
    "v_cmp_eq_u32 vcc, 12, v241\n" \
    "buffer_load_dwordx4 v[8:11], v36, s[40:43], s56 offen offset:128 \n" \
    "v_cmp_eq_u32 s[60:61], 3, v246\n" \
    "s_nop 0\n" \
    "v_cndmask_b32 v47, v248, v247, vcc\n" \
    "buffer_load_dwordx4 v[20:23], v37, s[40:43], s56 offen offset:128 \n" \
    "v_cndmask_b32 v51, 0, v244, s[60:61]\n" \
    "s_mov_b32 s67, 0x3b800000\n" \
    "s_mov_b32 s68, 0x47000000\n" \
    "buffer_load_dwordx4 v[32:35], v38, s[40:43], s56 offen offset:128 \n" \
    "s_mov_b32 s69, 0x7fff7fff\n" \
    "s_mov_b32 s70, 0x3f803f80\n" \
    "s_mov_b32 s58, 0x33333333\n" \
    "buffer_load_dwordx4 v[4:7], v36, s[40:43], s56 offen offset:64 \n" \
    "buffer_load_dwordx4 v[16:19], v37, s[40:43], s56 offen offset:64 \n" \
    "buffer_load_dwordx4 v[28:31], v38, s[40:43], s56 offen offset:64 \n" \
    "s_mov_b32 s59, 0x33333333\n" \
    "s_mov_b32 s55, 1\n" \
    "v_mov_b32 v95, 0\n" \
    "v_mov_b32 v96, 0\n" \
    "v_mov_b32 v103, 0\n" \
    "v_mov_b32 v104, 0\n" \
    "v_mov_b32 v111, 0\n" \
    "v_mov_b32 v112, 0\n" \
    "v_mov_b32 v119, 0\n" \
    "v_mov_b32 v120, 0\n" \
    "v_mov_b32 v127, 0\n" \
    "v_mov_b32 v128, 0\n" \
    "v_mov_b32 v135, 0\n" \
    "v_mov_b32 v136, 0\n" \
    "s_mov_b32 s60, 0x05040100\n" \
    "s_mov_b32 s61, 0x07060302\n" \
    "s_waitcnt vmcnt(17)\n" \
    "v_perm_b32 v56, v144, v140, s60\n" \
    "v_perm_b32 v57, v152, v148, s60\n" \
    "v_perm_b32 v58, v160, v156, s60\n" \
    "v_perm_b32 v59, v168, v164, s60\n" \
    "v_perm_b32 v60, v144, v140, s61\n" \
    "v_perm_b32 v61, v152, v148, s61\n" \
    "v_perm_b32 v62, v160, v156, s61\n" \
    "v_perm_b32 v63, v168, v164, s61\n" \
    "ds_write_b128 v232, v[56:59]\n" \
    "ds_write_b128 v232, v[60:63] offset:16\n" \
    "v_perm_b32 v64, v145, v141, s60\n" \
    "v_perm_b32 v65, v153, v149, s60\n" \
    "v_perm_b32 v66, v161, v157, s60\n" \
    "v_perm_b32 v67, v169, v165, s60\n" \
    "v_perm_b32 v68, v145, v141, s61\n" \
    "v_perm_b32 v69, v153, v149, s61\n" \
    "v_perm_b32 v70, v161, v157, s61\n" \
    "v_perm_b32 v71, v169, v165, s61\n" \
    "ds_write_b128 v233, v[64:67]\n" \
    "ds_write_b128 v233, v[68:71] offset:16\n" \
    "v_perm_b32 v56, v146, v142, s60\n" \
    "v_perm_b32 v57, v154, v150, s60\n" \
    "v_perm_b32 v58, v162, v158, s60\n" \
    "v_perm_b32 v59, v170, v166, s60\n" \
    "v_perm_b32 v60, v146, v142, s61\n" \
    "v_perm_b32 v61, v154, v150, s61\n" \
    "v_perm_b32 v62, v162, v158, s61\n" \
    "v_perm_b32 v63, v170, v166, s61\n" \
    "ds_write_b128 v234, v[56:59]\n" \
    "ds_write_b128 v234, v[60:63] offset:16\n" \
    "v_perm_b32 v64, v147, v143, s60\n" \
    "v_perm_b32 v65, v155, v151, s60\n" \
    "v_perm_b32 v66, v163, v159, s60\n" \
    "v_perm_b32 v67, v171, v167, s60\n" \
    "v_perm_b32 v68, v147, v143, s61\n" \
    "v_perm_b32 v69, v155, v151, s61\n" \
    "v_perm_b32 v70, v163, v159, s61\n" \
    "v_perm_b32 v71, v171, v167, s61\n" \
    "ds_write_b128 v235, v[64:67]\n" \
    "ds_write_b128 v235, v[68:71] offset:16\n" \
    "s_mov_b32 s60, 0x05040100\n" \
    "s_mov_b32 s61, 0x07060302\n" \
    "s_waitcnt vmcnt(9)\n" \
    "v_perm_b32 v56, v176, v172, s60\n" \
    "v_perm_b32 v57, v184, v180, s60\n" \
    "v_perm_b32 v58, v192, v188, s60\n" \
    "v_perm_b32 v59, v200, v196, s60\n" \
    "v_perm_b32 v60, v176, v172, s61\n" \
    "v_perm_b32 v61, v184, v180, s61\n" \
    "v_perm_b32 v62, v192, v188, s61\n" \
    "v_perm_b32 v63, v200, v196, s61\n" \
    "ds_write_b128 v232, v[56:59] offset:256\n" \
    "ds_write_b128 v232, v[60:63] offset:272\n" \
    "v_perm_b32 v64, v177, v173, s60\n" \
    "v_perm_b32 v65, v185, v181, s60\n" \
    "v_perm_b32 v66, v193, v189, s60\n" \
    "v_perm_b32 v67, v201, v197, s60\n" \
    "v_perm_b32 v68, v177, v173, s61\n" \
    "v_perm_b32 v69, v185, v181, s61\n" \
    "v_perm_b32 v70, v193, v189, s61\n" \
    "v_perm_b32 v71, v201, v197, s61\n" \
    "ds_write_b128 v233, v[64:67] offset:256\n" \
    "ds_write_b128 v233, v[68:71] offset:272\n" \
    "v_perm_b32 v56, v178, v174, s60\n" \
    "v_perm_b32 v57, v186, v182, s60\n" \
    "v_perm_b32 v58, v194, v190, s60\n" \
    "v_perm_b32 v59, v202, v198, s60\n" \
    "v_perm_b32 v60, v178, v174, s61\n" \
    "v_perm_b32 v61, v186, v182, s61\n" \
    "v_perm_b32 v62, v194, v190, s61\n" \
    "v_perm_b32 v63, v202, v198, s61\n" \
    "ds_write_b128 v234, v[56:59] offset:256\n" \
    "ds_write_b128 v234, v[60:63] offset:272\n" \
    "v_perm_b32 v64, v179, v175, s60\n" \
    "v_perm_b32 v65, v187, v183, s60\n" \
    "v_perm_b32 v66, v195, v191, s60\n" \
    "v_perm_b32 v67, v203, v199, s60\n" \
    "v_perm_b32 v68, v179, v175, s61\n" \
    "v_perm_b32 v69, v187, v183, s61\n" \
    "v_perm_b32 v70, v195, v191, s61\n" \
    "v_perm_b32 v71, v203, v199, s61\n" \
    "ds_write_b128 v235, v[64:67] offset:256\n" \
    "ds_write_b128 v235, v[68:71] offset:272\n" \
    "v_mov_b32 v246, 148736\n" \
    "v_mov_b32 v247, 1\n" \
    "s_mov_b64 exec, 1\n" \
    "ds_add_u32 v246, v247\n" \
    "s_mov_b64 exec, -1\n" \
    "s_waitcnt vmcnt(9)\n" \
    "v_lshlrev_b32 v52, 16, v249\n" \
    "s_mov_b32 s60, 0xc2c80000\n" \
    "v_mov_b32 v246, 0x40935dcc\n" \
    "v_med3_f32 v52, v52, s60, v246\n" \
    "v_cvt_pk_bf16_f32 v52, 0, v52\n" \
    "v_mul_f32 v52, 0x3fb8aa3b, v52\n" \
    "v_exp_f32 v52, v52\n" \
    "s_nop 0\n" \
    "v_cvt_pk_bf16_f32 v52, 0, v52\n" \
    "v_mul_f32 v52, 0x3fb8aa3b, v52\n" \
    "s_nop 1\n" \
    "v_readlane_b32 s36, v52, 0\n" \
    "v_readlane_b32 s37, v52, 16\n" \
    "v_readlane_b32 s38, v52, 32\n" \
    "v_readlane_b32 s39, v52, 48\n" \
    "s_mov_b32 s44, s92\n" \
    "s_and_b32 s45, s93, 0xffff\n" \
    "s_mov_b32 s46, 14002632\n" \
    "s_mov_b32 s47, 0x20000\n" \
    "s_min_u32 s66, s52, 2808\n" \
    "s_mul_i32 s60, s66, 19785\n" \
    "s_lshr_b32 s60, s60, 20\n" \
    "s_add_u32 s61, s66, s60\n" \
    "s_mul_i32 s62, s61, 4802\n" \
    "s_and_b32 s63, s62, 15\n" \
    "s_sub_u32 s62, s62, s63\n" \
    "s_add_u32 s64, s63, 690\n" \
    "s_add_u32 s65, s62, 4096\n" \
    "buffer_load_dwordx4 v[188:191], v39, s[44:47], s62 offen offset:1024\n" \
    "buffer_load_dwordx4 v[192:195], v39, s[44:47], s62 offen offset:2048\n" \
    "buffer_load_dwordx4 v[196:199], v39, s[44:47], s62 offen offset:3072\n" \
    "v_max_u32 v200, s63, v39\n" \
    "v_min_u32 v252, s64, v39\n" \
    "buffer_load_dwordx4 v[200:203], v200, s[44:47], s62 offen\n" \
    "buffer_load_dwordx4 v[252:255], v252, s[44:47], s65 offen\n" \
    "s_mov_b32 s76, 0xffff\n" \
    "s_mov_b32 s77, 0x0\n" \
    "s_mov_b32 s78, 0xffff0000\n" \
    "s_mov_b32 s79, 0x0\n" \
    "s_mov_b32 s80, 0x0\n" \
    "s_mov_b32 s81, 0xffff\n" \
    "s_mov_b32 s82, 0x0\n" \
    "s_mov_b32 s83, 0xffff0000\n" \
    "v_mov_b32 v246, 148736\n" \
    "s_branch Lwae%=\n" \
    "Lwa%=:\n" \
    "s_sleep 1\n" \
    "Lwae%=:\n" \
    "ds_read_b32 v247, v246\n" \
    "s_waitcnt lgkmcnt(0)\n" \
    "v_cmp_gt_u32 vcc, 4, v247\n" \
    "s_nop 1\n" \
    "s_cbranch_vccnz Lwa%=\n" \
    "s_waitcnt vmcnt(5)\n" \
    "Lgo%=:\n" \
    "s_max_u32 s60, s36, s37\n" \
    "s_max_u32 s61, s38, s39\n" \
    "s_max_u32 s60, s60, s61\n" \
    "s_cmp_gt_u32 s60, 0x420a7fac\n" \
    "s_cbranch_scc1 Lexit%=\n" \
    "s_cmp_ge_u32 s52, 2809\n" \
    "s_cbranch_scc1 Lalt%=\n" \
    "Ltop%=:\n" \
    "s_cmp_gt_u32 s54, 1\n" \
    "s_cselect_b32 s60, s53, 0\n" \
    "s_cselect_b32 s42, s42, 0\n" \
    "s_cselect_b32 s46, s46, 0\n" \
    "s_add_u32 s66, s52, s60\n" \
    "ds_read_b128 v[228:231], v39\n" \
    "ds_read_b128 v[232:235], v39 offset:1040\n" \
    "ds_read_b128 v[236:239], v39 offset:2080\n" \
    "s_mul_i32 s60, s66, 19785\n" \
    "s_lshr_b32 s60, s60, 20\n" \
    "s_add_u32 s61, s66, s60\n" \
    "s_mul_i32 s62, s61, 4802\n" \
    "s_and_b32 s63, s62, 15\n" \
    "s_sub_u32 s62, s62, s63\n" \
    "s_add_u32 s64, s63, 690\n" \
    "s_add_u32 s65, s62, 4096\n" \
    "s_mul_i32 s61, s60, 436800\n" \
    "s_mul_i32 s57, s66, 1344\n" \
    "s_add_u32 s57, s57, s61\n" \
    "s_add_u32 s57, s57, 218304\n" \
    "ds_read_b128 v[176:179], v39 offset:3120\n" \
    "ds_read_b128 v[180:183], v39 offset:4160\n" \
    "ds_read_b128 v[184:187], v39 offset:5200\n" \
    "s_waitcnt vmcnt(9) lgkmcnt(5)\n" \
    "v_mfma_f32_16x16x32_bf16 v[204:207], v[228:231], v[0:3], 0\n" \
    "v_mfma_f32_16x16x32_bf16 v[208:211], v[228:231], v[12:15], 0\n" \
    "v_mfma_f32_16x16x32_bf16 v[212:215], v[228:231], v[24:27], 0\n" \
    "ds_read_b128 v[228:231], v39 offset:6240\n" \
    "s_waitcnt lgkmcnt(4)\n" \
    "v_mfma_f32_16x16x32_bf16 v[204:207], v[232:235], v[4:7], v[204:207]\n" \
    "v_mfma_f32_16x16x32_bf16 v[208:211], v[232:235], v[16:19], v[208:211]\n" \
    "v_mfma_f32_16x16x32_bf16 v[212:215], v[232:235], v[28:31], v[212:215]\n" \
    "ds_read_b128 v[232:235], v39 offset:7280\n" \
    "v_mfma_f32_16x16x32_bf16 v[204:207], v[236:239], v[8:11], v[204:207]\n" \
    "v_mfma_f32_16x16x32_bf16 v[208:211], v[236:239], v[20:23], v[208:211]\n" \
    "v_mfma_f32_16x16x32_bf16 v[212:215], v[236:239], v[32:35], v[212:215]\n" \
    "ds_read_b128 v[236:239], v39 offset:8320\n" \
    "v_mov_b32 v55, s70\n" \
    "s_waitcnt lgkmcnt(3)\n" \
    "v_mfma_f32_16x16x32_bf16 v[216:219], v[176:179], v[0:3], 0\n" \
    "s_nop 1\n" \
    "v_cvt_pk_f16_f32 v56, v204, v205\n" \
    "v_mfma_f32_16x16x32_bf16 v[220:223], v[176:179], v[12:15], 0\n" \
    "v_pk_mul_f16 v240, v56, v56\n" \
    "v_cvt_pk_f16_f32 v57, v206, v207\n" \
    "v_mfma_f32_16x16x32_bf16 v[224:227], v[176:179], v[24:27], 0\n" \
    "ds_read_b128 v[176:179], v39 offset:9360\n" \
    "v_pk_fma_f16 v240, v57, v57, v240\n" \
    "v_mfma_f32_16x16x32_bf16 v[216:219], v[180:183], v[4:7], v[216:219]\n" \
    "v_cvt_pk_f16_f32 v68, v208, v209\n" \
    "v_pk_mul_f16 v244, v68, v68\n" \
    "v_mfma_f32_16x16x32_bf16 v[220:223], v[180:183], v[16:19], v[220:223]\n" \
    "v_cvt_pk_f16_f32 v69, v210, v211\n" \
    "v_pk_fma_f16 v244, v69, v69, v244\n" \
    "v_mfma_f32_16x16x32_bf16 v[224:227], v[180:183], v[28:31], v[224:227]\n" \
    "ds_read_b128 v[180:183], v39 offset:10400\n" \
    "v_cvt_pk_f16_f32 v80, v212, v213\n" \
    "v_mfma_f32_16x16x32_bf16 v[216:219], v[184:187], v[8:11], v[216:219]\n" \
    "v_pk_mul_f16 v248, v80, v80\n" \
    "v_cvt_pk_f16_f32 v81, v214, v215\n" \
    "v_mfma_f32_16x16x32_bf16 v[220:223], v[184:187], v[20:23], v[220:223]\n" \
    "v_pk_fma_f16 v248, v81, v81, v248\n" \
    "v_mfma_f32_16x16x32_bf16 v[224:227], v[184:187], v[32:35], v[224:227]\n" \
    "ds_read_b128 v[184:187], v39 offset:11440\n" \
    "s_waitcnt lgkmcnt(3)\n" \
    "v_mfma_f32_16x16x32_bf16 v[204:207], v[228:231], v[0:3], 0\n" \
    "v_cvt_pk_f16_f32 v58, v216, v217\n" \
    "v_pk_fma_f16 v240, v58, v58, v240\n" \
    "v_mfma_f32_16x16x32_bf16 v[208:211], v[228:231], v[12:15], 0\n" \
    "v_cvt_pk_f16_f32 v59, v218, v219\n" \
    "v_pk_mul_f16 v241, v59, v59\n" \
    "v_mfma_f32_16x16x32_bf16 v[212:215], v[228:231], v[24:27], 0\n" \
    "ds_read_b128 v[228:231], v39 offset:12480\n" \
    "v_cvt_pk_f16_f32 v70, v220, v221\n" \
    "v_mfma_f32_16x16x32_bf16 v[204:207], v[232:235], v[4:7], v[204:207]\n" \
    "v_pk_fma_f16 v244, v70, v70, v244\n" \
    "v_cvt_pk_f16_f32 v71, v222, v223\n" \
    "v_mfma_f32_16x16x32_bf16 v[208:211], v[232:235], v[16:19], v[208:211]\n" \
    "v_pk_mul_f16 v245, v71, v71\n" \
    "v_cvt_pk_f16_f32 v82, v224, v225\n" \
    "v_mfma_f32_16x16x32_bf16 v[212:215], v[232:235], v[28:31], v[212:215]\n" \
    "ds_read_b128 v[232:235], v39 offset:13520\n" \
    "v_pk_fma_f16 v248, v82, v82, v248\n" \
    "v_mfma_f32_16x16x32_bf16 v[204:207], v[236:239], v[8:11], v[204:207]\n" \
    "v_cvt_pk_f16_f32 v83, v226, v227\n" \
    "v_pk_mul_f16 v249, v83, v83\n" \
    "v_mfma_f32_16x16x32_bf16 v[208:211], v[236:239], v[20:23], v[208:211]\n" \
    "v_mfma_f32_16x16x32_bf16 v[212:215], v[236:239], v[32:35], v[212:215]\n" \
    "ds_read_b128 v[236:239], v39 offset:14560\n" \
    "s_waitcnt lgkmcnt(3)\n" \
    "v_mfma_f32_16x16x32_bf16 v[216:219], v[176:179], v[0:3], 0\n" \
    "s_nop 0\n" \
    "v_cvt_pk_f16_f32 v60, v204, v205\n" \
    "v_mfma_f32_16x16x32_bf16 v[220:223], v[176:179], v[12:15], 0\n" \
    "v_pk_fma_f16 v241, v60, v60, v241\n" \
    "v_cvt_pk_f16_f32 v61, v206, v207\n" \
    "v_mfma_f32_16x16x32_bf16 v[224:227], v[176:179], v[24:27], 0\n" \
    "ds_read_b128 v[176:179], v39 offset:15600\n" \
    "v_pk_fma_f16 v241, v61, v61, v241\n" \
    "v_mfma_f32_16x16x32_bf16 v[216:219], v[180:183], v[4:7], v[216:219]\n" \
    "v_cvt_pk_f16_f32 v72, v208, v209\n" \
    "v_pk_fma_f16 v245, v72, v72, v245\n" \
    "v_mfma_f32_16x16x32_bf16 v[220:223], v[180:183], v[16:19], v[220:223]\n" \
    "v_cvt_pk_f16_f32 v73, v210, v211\n" \
    "v_pk_fma_f16 v245, v73, v73, v245\n" \
    "v_mfma_f32_16x16x32_bf16 v[224:227], v[180:183], v[28:31], v[224:227]\n" \
    "ds_read_b128 v[180:183], v39 offset:16640\n" \
    "v_cvt_pk_f16_f32 v84, v212, v213\n" \
    "v_mfma_f32_16x16x32_bf16 v[216:219], v[184:187], v[8:11], v[216:219]\n" \
    "v_pk_fma_f16 v249, v84, v84, v249\n" \
    "v_cvt_pk_f16_f32 v85, v214, v215\n" \
    "v_mfma_f32_16x16x32_bf16 v[220:223], v[184:187], v[20:23], v[220:223]\n" \
    "v_pk_fma_f16 v249, v85, v85, v249\n" \
    "v_mfma_f32_16x16x32_bf16 v[224:227], v[184:187], v[32:35], v[224:227]\n" \
    "ds_read_b128 v[184:187], v39 offset:17680\n" \
    "s_waitcnt lgkmcnt(3)\n" \
    "v_mfma_f32_16x16x32_bf16 v[204:207], v[228:231], v[0:3], 0\n" \
    "v_cvt_pk_f16_f32 v62, v216, v217\n" \
    "v_pk_mul_f16 v242, v62, v62\n" \
    "v_mfma_f32_16x16x32_bf16 v[208:211], v[228:231], v[12:15], 0\n" \
    "v_cvt_pk_f16_f32 v63, v218, v219\n" \
    "v_pk_fma_f16 v242, v63, v63, v242\n" \
    "v_mfma_f32_16x16x32_bf16 v[212:215], v[228:231], v[24:27], 0\n" \
    "ds_read_b128 v[228:231], v39 offset:18720\n" \
    "v_cvt_pk_f16_f32 v74, v220, v221\n" \
    "v_mfma_f32_16x16x32_bf16 v[204:207], v[232:235], v[4:7], v[204:207]\n" \
    "v_pk_mul_f16 v246, v74, v74\n" \
    "v_cvt_pk_f16_f32 v75, v222, v223\n" \
    "v_mfma_f32_16x16x32_bf16 v[208:211], v[232:235], v[16:19], v[208:211]\n" \
    "v_pk_fma_f16 v246, v75, v75, v246\n" \
    "v_cvt_pk_f16_f32 v86, v224, v225\n" \
    "v_mfma_f32_16x16x32_bf16 v[212:215], v[232:235], v[28:31], v[212:215]\n" \
    "ds_read_b128 v[232:235], v39 offset:19760\n" \
    "v_pk_mul_f16 v250, v86, v86\n" \
    "v_mfma_f32_16x16x32_bf16 v[204:207], v[236:239], v[8:11], v[204:207]\n" \
    "v_cvt_pk_f16_f32 v87, v226, v227\n" \
    "v_pk_fma_f16 v250, v87, v87, v250\n" \
    "v_mfma_f32_16x16x32_bf16 v[208:211], v[236:239], v[20:23], v[208:211]\n" \
    "v_mfma_f32_16x16x32_bf16 v[212:215], v[236:239], v[32:35], v[212:215]\n" \
    "ds_read_b128 v[236:239], v39 offset:20800\n" \
    "s_waitcnt lgkmcnt(3)\n" \
    "v_mfma_f32_16x16x32_bf16 v[216:219], v[176:179], v[0:3], 0\n" \
    "s_nop 0\n" \
    "v_cvt_pk_f16_f32 v64, v204, v205\n" \
    "v_mfma_f32_16x16x32_bf16 v[220:223], v[176:179], v[12:15], 0\n" \
    "v_pk_fma_f16 v242, v64, v64, v242\n" \
    "v_cvt_pk_f16_f32 v65, v206, v207\n" \
    "v_mfma_f32_16x16x32_bf16 v[224:227], v[176:179], v[24:27], 0\n" \
    "ds_read_b128 v[176:179], v39 offset:21840\n" \
    "v_pk_mul_f16 v243, v65, v65\n" \
    "v_mfma_f32_16x16x32_bf16 v[216:219], v[180:183], v[4:7], v[216:219]\n" \
    "v_cvt_pk_f16_f32 v76, v208, v209\n" \
    "v_pk_fma_f16 v246, v76, v76, v246\n" \
    "v_mfma_f32_16x16x32_bf16 v[220:223], v[180:183], v[16:19], v[220:223]\n" \
    "v_cvt_pk_f16_f32 v77, v210, v211\n" \
    "v_pk_mul_f16 v247, v77, v77\n" \
    "v_mfma_f32_16x16x32_bf16 v[224:227], v[180:183], v[28:31], v[224:227]\n" \
    "ds_read_b128 v[180:183], v39 offset:22880\n" \
    "v_cvt_pk_f16_f32 v88, v212, v213\n" \
    "v_mfma_f32_16x16x32_bf16 v[216:219], v[184:187], v[8:11], v[216:219]\n" \
    "v_pk_fma_f16 v250, v88, v88, v250\n" \
    "v_cvt_pk_f16_f32 v89, v214, v215\n" \
    "v_mfma_f32_16x16x32_bf16 v[220:223], v[184:187], v[20:23], v[220:223]\n" \
    "v_pk_mul_f16 v251, v89, v89\n" \
    "v_mfma_f32_16x16x32_bf16 v[224:227], v[184:187], v[32:35], v[224:227]\n" \
    "ds_read_b128 v[184:187], v39 offset:23920\n" \
    "s_waitcnt lgkmcnt(3)\n" \
    "v_mfma_f32_16x16x32_bf16 v[204:207], v[228:231], v[0:3], 0\n" \
    "v_cvt_pk_f16_f32 v66, v216, v217\n" \
    "v_pk_fma_f16 v243, v66, v66, v243\n" \
    "v_mfma_f32_16x16x32_bf16 v[208:211], v[228:231], v[12:15], 0\n" \
    "v_cvt_pk_f16_f32 v67, v218, v219\n" \
    "v_pk_fma_f16 v243, v67, v67, v243\n" \
    "v_mfma_f32_16x16x32_bf16 v[212:215], v[228:231], v[24:27], 0\n" \
    "ds_read_b128 v[228:231], v39 offset:24960\n" \
    "v_cvt_pk_f16_f32 v78, v220, v221\n" \
    "v_mfma_f32_16x16x32_f16 v[140:143], v[48:51], v[240:243], 0\n" \
    "v_pk_fma_f16 v247, v78, v78, v247\n" \
    "v_cvt_pk_f16_f32 v79, v222, v223\n" \
    "v_mfma_f32_16x16x32_bf16 v[204:207], v[232:235], v[4:7], v[204:207]\n" \
    "v_pk_fma_f16 v247, v79, v79, v247\n" \
    "v_cvt_pk_f16_f32 v90, v224, v225\n" \
    "v_mfma_f32_16x16x32_bf16 v[208:211], v[232:235], v[16:19], v[208:211]\n" \
    "v_pk_fma_f16 v251, v90, v90, v251\n" \
    "v_cvt_pk_f16_f32 v91, v226, v227\n" \
    "v_mfma_f32_16x16x32_f16 v[144:147], v[48:51], v[244:247], 0\n" \
    "v_pk_fma_f16 v251, v91, v91, v251\n" \
    "v_rsq_f32 v141, v140\n" \
    "v_mfma_f32_16x16x32_bf16 v[212:215], v[232:235], v[28:31], v[212:215]\n" \
    "ds_read_b128 v[232:235], v39 offset:26000\n" \
    "v_cvt_f16_f32 v141, v141\n" \
    "v_mfma_f32_16x16x32_f16 v[148:151], v[48:51], v[248:251], 0\n" \
    "ds_bpermute_b32 v164, v43, v141\n" \
    "ds_bpermute_b32 v165, v43, v141 offset:64\n" \
    "v_mfma_f32_16x16x32_bf16 v[204:207], v[236:239], v[8:11], v[204:207]\n" \
    "ds_bpermute_b32 v166, v43, v141 offset:128\n" \
    "ds_bpermute_b32 v167, v43, v141 offset:192\n" \
    "v_mfma_f32_16x16x32_bf16 v[208:211], v[236:239], v[20:23], v[208:211]\n" \
    "v_rsq_f32 v145, v144\n" \
    "v_mfma_f32_16x16x32_bf16 v[212:215], v[236:239], v[32:35], v[212:215]\n" \
    "ds_read_b128 v[236:239], v39 offset:27040\n" \
    "v_cvt_f16_f32 v145, v145\n" \
    "s_waitcnt lgkmcnt(6)\n" \
    "v_mfma_f32_16x16x32_bf16 v[216:219], v[176:179], v[0:3], 0\n" \
    "ds_bpermute_b32 v168, v43, v145\n" \
    "ds_bpermute_b32 v169, v43, v145 offset:64\n" \
    "v_mfma_f32_16x16x32_bf16 v[220:223], v[176:179], v[12:15], 0\n" \
    "ds_bpermute_b32 v170, v43, v145 offset:128\n" \
    "ds_bpermute_b32 v171, v43, v145 offset:192\n" \
    "v_mfma_f32_16x16x32_bf16 v[224:227], v[176:179], v[24:27], 0\n" \
    "ds_read_b128 v[176:179], v39 offset:28080\n" \
    "v_rsq_f32 v149, v148\n" \
    "v_mfma_f32_16x16x32_bf16 v[216:219], v[180:183], v[4:7], v[216:219]\n" \
    "v_cvt_f16_f32 v149, v149\n" \
    "s_waitcnt lgkmcnt(8)\n" \
    "v_pk_mul_f16 v56, v56, v164 op_sel_hi:[1,0]\n" \
    "v_mfma_f32_16x16x32_bf16 v[220:223], v[180:183], v[16:19], v[220:223]\n" \
    "ds_bpermute_b32 v172, v43, v149\n" \
    "ds_bpermute_b32 v173, v43, v149 offset:64\n" \
    "v_mfma_f32_16x16x32_bf16 v[224:227], v[180:183], v[28:31], v[224:227]\n" \
    "ds_bpermute_b32 v174, v43, v149 offset:128\n" \
    "ds_bpermute_b32 v175, v43, v149 offset:192\n" \
    "v_mfma_f32_16x16x32_bf16 v[216:219], v[184:187], v[8:11], v[216:219]\n" \
    "ds_read_b128 v[180:183], v39 offset:29120\n" \
    "v_pk_mul_f16 v57, v57, v164 op_sel_hi:[1,0]\n" \
    "v_mfma_f32_16x16x32_bf16 v[220:223], v[184:187], v[20:23], v[220:223]\n" \
    "v_pk_mul_f16 v58, v58, v164 op_sel_hi:[1,0]\n" \
    "v_pk_mul_f16 v59, v59, v165 op_sel_hi:[1,0]\n" \
    "v_mfma_f32_16x16x32_bf16 v[224:227], v[184:187], v[32:35], v[224:227]\n" \
    "v_pk_mul_f16 v60, v60, v165 op_sel_hi:[1,0]\n" \
    "v_pk_mul_f16 v61, v61, v165 op_sel_hi:[1,0]\n" \
    "s_waitcnt lgkmcnt(5)\n" \
    "v_pk_mul_f16 v62, v62, v166 op_sel_hi:[1,0]\n" \
    "v_pk_mul_f16 v63, v63, v166 op_sel_hi:[1,0]\n" \
    "v_pk_mul_f16 v64, v64, v166 op_sel_hi:[1,0]\n" \
    "v_pk_mul_f16 v65, v65, v167 op_sel_hi:[1,0]\n" \
    "v_pk_mul_f16 v66, v66, v167 op_sel_hi:[1,0]\n" \
    "v_pk_mul_f16 v67, v67, v167 op_sel_hi:[1,0]\n" \
    "v_pk_mul_f16 v68, v68, v168 op_sel_hi:[1,0]\n" \
    "v_pk_mul_f16 v69, v69, v168 op_sel_hi:[1,0]\n" \
    "v_pk_mul_f16 v70, v70, v168 op_sel_hi:[1,0]\n" \
    "v_cvt_pk_f16_f32 v92, v204, v205\n" \
    "v_pk_mul_f16 v71, v71, v169 op_sel_hi:[1,0]\n" \
    "v_pk_mul_f16 v240, v92, v92\n" \
    "v_pk_mul_f16 v72, v72, v169 op_sel_hi:[1,0]\n" \
    "v_cvt_pk_f16_f32 v93, v206, v207\n" \
    "v_mfma_f32_16x16x32_bf16 v[204:207], v[228:231], v[0:3], 0\n" \
    "ds_read_b128 v[184:187], v39 offset:30160\n" \
    "v_pk_mul_f16 v73, v73, v169 op_sel_hi:[1,0]\n" \
    "v_mfma_f32_16x16x32_bf16 v[204:207], v[232:235], v[4:7], v[204:207]\n" \
    "v_pk_fma_f16 v240, v93, v93, v240\n" \
    "v_pk_mul_f16 v74, v74, v170 op_sel_hi:[1,0]\n" \
    "v_mfma_f32_16x16x32_bf16 v[204:207], v[236:239], v[8:11], v[204:207]\n" \
    "v_cvt_pk_f16_f32 v108, v208, v209\n" \
    "v_pk_mul_f16 v75, v75, v170 op_sel_hi:[1,0]\n" \
    "v_pk_mul_f16 v244, v108, v108\n" \
    "v_pk_mul_f16 v76, v76, v170 op_sel_hi:[1,0]\n" \
    "v_cvt_pk_f16_f32 v109, v210, v211\n" \
    "v_mfma_f32_16x16x32_bf16 v[208:211], v[228:231], v[12:15], 0\n" \
    "v_pk_mul_f16 v77, v77, v171 op_sel_hi:[1,0]\n" \
    "v_pk_fma_f16 v244, v109, v109, v244\n" \
    "v_mfma_f32_16x16x32_bf16 v[208:211], v[232:235], v[16:19], v[208:211]\n" \
    "v_pk_mul_f16 v78, v78, v171 op_sel_hi:[1,0]\n" \
    "v_cvt_pk_f16_f32 v124, v212, v213\n" \
    "v_mfma_f32_16x16x32_bf16 v[208:211], v[236:239], v[20:23], v[208:211]\n" \
    "v_pk_mul_f16 v79, v79, v171 op_sel_hi:[1,0]\n" \
    "v_pk_mul_f16 v248, v124, v124\n" \
    "s_waitcnt lgkmcnt(1)\n" \
    "v_pk_mul_f16 v80, v80, v172 op_sel_hi:[1,0]\n" \
    "v_cvt_pk_f16_f32 v125, v214, v215\n" \
    "v_mfma_f32_16x16x32_bf16 v[212:215], v[228:231], v[24:27], 0\n" \
    "ds_read_b128 v[228:231], v39 offset:31200\n" \
    "v_pk_mul_f16 v81, v81, v172 op_sel_hi:[1,0]\n" \
    "v_mfma_f32_16x16x32_bf16 v[212:215], v[232:235], v[28:31], v[212:215]\n" \
    "ds_read_b128 v[232:235], v39 offset:32240\n" \
    "v_pk_fma_f16 v248, v125, v125, v248\n" \
    "v_mfma_f32_16x16x32_bf16 v[212:215], v[236:239], v[32:35], v[212:215]\n" \
    "ds_read_b128 v[236:239], v39 offset:33280\n" \
    "v_pk_mul_f16 v82, v82, v172 op_sel_hi:[1,0]\n" \
    "v_pk_mul_f16 v83, v83, v173 op_sel_hi:[1,0]\n" \
    "v_pk_mul_f16 v84, v84, v173 op_sel_hi:[1,0]\n" \
    "v_pk_mul_f16 v85, v85, v173 op_sel_hi:[1,0]\n" \
    "v_pk_mul_f16 v86, v86, v174 op_sel_hi:[1,0]\n" \
    "v_pk_mul_f16 v87, v87, v174 op_sel_hi:[1,0]\n" \
    "v_pk_mul_f16 v88, v88, v174 op_sel_hi:[1,0]\n" \
    "v_pk_mul_f16 v89, v89, v175 op_sel_hi:[1,0]\n" \
    "v_pk_mul_f16 v90, v90, v175 op_sel_hi:[1,0]\n" \
    "v_pk_mul_f16 v91, v91, v175 op_sel_hi:[1,0]\n" \
    "v_cvt_pk_f16_f32 v94, v216, v217\n" \
    "v_pk_fma_f16 v240, v94, v94, v240\n" \
    "v_cvt_pk_f16_f32 v97, v218, v219\n" \
    "v_mfma_f32_16x16x32_bf16 v[216:219], v[176:179], v[0:3], 0\n" \
    "v_pk_mul_f16 v241, v97, v97\n" \
    "v_cvt_pk_f16_f32 v110, v220, v221\n" \
    "v_mfma_f32_16x16x32_bf16 v[216:219], v[180:183], v[4:7], v[216:219]\n" \
    "v_pk_fma_f16 v244, v110, v110, v244\n" \
    "v_cvt_pk_f16_f32 v113, v222, v223\n" \
    "v_mfma_f32_16x16x32_bf16 v[220:223], v[176:179], v[12:15], 0\n" \
    "v_pk_mul_f16 v245, v113, v113\n" \
    "v_cvt_pk_f16_f32 v126, v224, v225\n" \
    "v_mfma_f32_16x16x32_bf16 v[220:223], v[180:183], v[16:19], v[220:223]\n" \
    "v_pk_fma_f16 v248, v126, v126, v248\n" \
    "v_cvt_pk_f16_f32 v129, v226, v227\n" \
    "v_mfma_f32_16x16x32_bf16 v[224:227], v[176:179], v[24:27], 0\n" \
    "ds_read_b128 v[176:179], v39 offset:34320\n" \
    "v_pk_mul_f16 v249, v129, v129\n" \
    "v_mfma_f32_16x16x32_bf16 v[224:227], v[180:183], v[28:31], v[224:227]\n" \
    "ds_read_b128 v[180:183], v39 offset:35360\n" \
    "v_cvt_pk_f16_f32 v98, v204, v205\n" \
    "s_waitcnt lgkmcnt(2)\n" \
    "v_mfma_f32_16x16x32_bf16 v[216:219], v[184:187], v[8:11], v[216:219]\n" \
    "v_pk_fma_f16 v241, v98, v98, v241\n" \
    "v_cvt_pk_f16_f32 v99, v206, v207\n" \
    "v_mfma_f32_16x16x32_bf16 v[220:223], v[184:187], v[20:23], v[220:223]\n" \
    "v_pk_fma_f16 v241, v99, v99, v241\n" \
    "v_cvt_pk_f16_f32 v114, v208, v209\n" \
    "v_mfma_f32_16x16x32_bf16 v[224:227], v[184:187], v[32:35], v[224:227]\n" \
    "ds_read_b128 v[184:187], v39 offset:36400\n" \
    "v_pk_fma_f16 v245, v114, v114, v245\n" \
    "v_mfma_f32_16x16x32_bf16 v[204:207], v[228:231], v[0:3], 0\n" \
    "v_cvt_pk_f16_f32 v115, v210, v211\n" \
    "v_pk_fma_f16 v245, v115, v115, v245\n" \
    "v_mfma_f32_16x16x32_bf16 v[208:211], v[228:231], v[12:15], 0\n" \
    "v_cvt_pk_f16_f32 v130, v212, v213\n" \
    "v_pk_fma_f16 v249, v130, v130, v249\n" \
    "v_mfma_f32_16x16x32_bf16 v[204:207], v[232:235], v[4:7], v[204:207]\n" \
    "v_cvt_pk_f16_f32 v131, v214, v215\n" \
    "v_pk_fma_f16 v249, v131, v131, v249\n" \
    "v_mfma_f32_16x16x32_bf16 v[212:215], v[228:231], v[24:27], 0\n" \
    "ds_read_b128 v[228:231], v39 offset:37440\n" \
    "v_cvt_pk_f16_f32 v100, v216, v217\n" \
    "v_mfma_f32_16x16x32_bf16 v[208:211], v[232:235], v[16:19], v[208:211]\n" \
    "v_pk_mul_f16 v242, v100, v100\n" \
    "v_cvt_pk_f16_f32 v101, v218, v219\n" \
    "v_mfma_f32_16x16x32_bf16 v[212:215], v[232:235], v[28:31], v[212:215]\n" \
    "ds_read_b128 v[232:235], v39 offset:38480\n" \
    "v_pk_fma_f16 v242, v101, v101, v242\n" \
    "v_mfma_f32_16x16x32_bf16 v[204:207], v[236:239], v[8:11], v[204:207]\n" \
    "v_cvt_pk_f16_f32 v116, v220, v221\n" \
    "v_pk_mul_f16 v246, v116, v116\n" \
    "v_mfma_f32_16x16x32_bf16 v[208:211], v[236:239], v[20:23], v[208:211]\n" \
    "v_cvt_pk_f16_f32 v117, v222, v223\n" \
    "v_pk_fma_f16 v246, v117, v117, v246\n" \
    "v_mfma_f32_16x16x32_bf16 v[212:215], v[236:239], v[32:35], v[212:215]\n" \
    "ds_read_b128 v[236:239], v39 offset:39520\n" \
    "v_cvt_pk_f16_f32 v132, v224, v225\n" \
    "s_waitcnt lgkmcnt(3)\n" \
    "v_mfma_f32_16x16x32_bf16 v[216:219], v[176:179], v[0:3], 0\n" \
    "v_pk_mul_f16 v250, v132, v132\n" \
    "v_cvt_pk_f16_f32 v133, v226, v227\n" \
    "v_mfma_f32_16x16x32_bf16 v[220:223], v[176:179], v[12:15], 0\n" \
    "v_pk_fma_f16 v250, v133, v133, v250\n" \
    "v_cvt_pk_f16_f32 v102, v204, v205\n" \
    "v_mfma_f32_16x16x32_bf16 v[224:227], v[176:179], v[24:27], 0\n" \
    "ds_read_b128 v[176:179], v39 offset:40560\n" \
    "v_pk_fma_f16 v242, v102, v102, v242\n" \
    "v_mfma_f32_16x16x32_bf16 v[216:219], v[180:183], v[4:7], v[216:219]\n" \
    "v_cvt_pk_f16_f32 v105, v206, v207\n" \
    "v_pk_mul_f16 v243, v105, v105\n" \
    "v_mfma_f32_16x16x32_bf16 v[220:223], v[180:183], v[16:19], v[220:223]\n" \
    "v_cvt_pk_f16_f32 v118, v208, v209\n" \
    "v_pk_fma_f16 v246, v118, v118, v246\n" \
    "v_mfma_f32_16x16x32_bf16 v[224:227], v[180:183], v[28:31], v[224:227]\n" \
    "ds_read_b128 v[180:183], v39 offset:41600\n" \
    "v_cvt_pk_f16_f32 v121, v210, v211\n" \
    "v_mfma_f32_16x16x32_bf16 v[216:219], v[184:187], v[8:11], v[216:219]\n" \
    "v_pk_mul_f16 v247, v121, v121\n" \
    "v_cvt_pk_f16_f32 v134, v212, v213\n" \
    "v_mfma_f32_16x16x32_bf16 v[220:223], v[184:187], v[20:23], v[220:223]\n" \
    "v_pk_fma_f16 v250, v134, v134, v250\n" \
    "v_cvt_pk_f16_f32 v137, v214, v215\n" \
    "v_mfma_f32_16x16x32_bf16 v[224:227], v[184:187], v[32:35], v[224:227]\n" \
    "ds_read_b128 v[184:187], v39 offset:42640\n" \
    "v_pk_mul_f16 v251, v137, v137\n" \
    "s_waitcnt lgkmcnt(3)\n" \
    "v_mfma_f32_16x16x32_bf16 v[204:207], v[0:3], v[228:231], 0\n" \
    "v_cvt_pk_f16_f32 v106, v216, v217\n" \
    "v_pk_fma_f16 v243, v106, v106, v243\n" \
    "v_mfma_f32_16x16x32_bf16 v[208:211], v[12:15], v[228:231], 0\n" \
    "v_cvt_pk_f16_f32 v107, v218, v219\n" \
    "v_pk_fma_f16 v243, v107, v107, v243\n" \
    "v_mfma_f32_16x16x32_bf16 v[212:215], v[24:27], v[228:231], 0\n" \
    "ds_read_b128 v[228:231], v39 offset:43680\n" \
    "v_cvt_pk_f16_f32 v122, v220, v221\n" \
    "v_mfma_f32_16x16x32_f16 v[152:155], v[48:51], v[240:243], 0\n" \
    "ds_read_b128 v[240:243], v39 offset:46800\n" \
    "v_pk_fma_f16 v247, v122, v122, v247\n" \
    "v_mfma_f32_16x16x32_bf16 v[204:207], v[4:7], v[232:235], v[204:207]\n" \
    "v_cvt_pk_f16_f32 v123, v222, v223\n" \
    "v_pk_fma_f16 v247, v123, v123, v247\n" \
    "v_mfma_f32_16x16x32_bf16 v[208:211], v[16:19], v[232:235], v[208:211]\n" \
    "v_cvt_pk_f16_f32 v138, v224, v225\n" \
    "v_pk_fma_f16 v251, v138, v138, v251\n" \
    "v_mfma_f32_16x16x32_f16 v[156:159], v[48:51], v[244:247], 0\n" \
    "ds_read_b128 v[244:247], v39 offset:47840\n" \
    "v_cvt_pk_f16_f32 v139, v226, v227\n" \
    "v_mfma_f32_16x16x32_bf16 v[212:215], v[28:31], v[232:235], v[212:215]\n" \
    "ds_read_b128 v[232:235], v39 offset:44720\n" \
    "v_pk_fma_f16 v251, v139, v139, v251\n" \
    "v_mfma_f32_16x16x32_bf16 v[204:207], v[8:11], v[236:239], v[204:207]\n" \
    "v_rsq_f32 v153, v152\n" \
    "v_mfma_f32_16x16x32_f16 v[160:163], v[48:51], v[248:251], 0\n" \
    "ds_read_b128 v[248:251], v39 offset:48880\n" \
    "v_mul_f32 v153, v153, v52\n" \
    "v_mfma_f32_16x16x32_bf16 v[208:211], v[20:23], v[236:239], v[208:211]\n" \
    "v_cvt_f16_f32 v153, v153\n" \
    "v_rsq_f32 v157, v156\n" \
    "v_mfma_f32_16x16x32_bf16 v[212:215], v[32:35], v[236:239], v[212:215]\n" \
    "ds_bpermute_b32 v164, v43, v153\n" \
    "ds_bpermute_b32 v165, v43, v153 offset:64\n" \
    "s_waitcnt lgkmcnt(5)\n" \
    "v_mfma_f32_16x16x32_bf16 v[216:219], v[0:3], v[176:179], 0\n" \
    "ds_bpermute_b32 v166, v43, v153 offset:128\n" \
    "ds_bpermute_b32 v167, v43, v153 offset:192\n" \
    "v_mfma_f32_16x16x32_bf16 v[220:223], v[12:15], v[176:179], 0\n" \
    "ds_read_b128 v[236:239], v39 offset:45760\n" \
    "v_mul_f32 v157, v157, v52\n" \
    "v_mfma_f32_16x16x32_bf16 v[224:227], v[24:27], v[176:179], 0\n" \
    "v_cvt_f16_f32 v157, v157\n" \
    "v_rsq_f32 v161, v160\n" \
    "v_mfma_f32_16x16x32_bf16 v[216:219], v[4:7], v[180:183], v[216:219]\n" \
    "ds_bpermute_b32 v168, v43, v157\n" \
    "ds_bpermute_b32 v169, v43, v157 offset:64\n" \
    "v_mfma_f32_16x16x32_bf16 v[220:223], v[16:19], v[180:183], v[220:223]\n" \
    "ds_bpermute_b32 v170, v43, v157 offset:128\n" \
    "ds_bpermute_b32 v171, v43, v157 offset:192\n" \
    "v_mfma_f32_16x16x32_bf16 v[224:227], v[28:31], v[180:183], v[224:227]\n" \
    "v_mul_f32 v161, v161, v52\n" \
    "v_cvt_f16_f32 v161, v161\n" \
    "v_mfma_f32_16x16x32_bf16 v[216:219], v[8:11], v[184:187], v[216:219]\n" \
    "ds_bpermute_b32 v172, v43, v161\n" \
    "s_waitcnt lgkmcnt(8)\n" \
    "v_pk_mul_f16 v92, v92, v164 op_sel_hi:[1,0]\n" \
    "v_mfma_f32_16x16x32_bf16 v[220:223], v[20:23], v[184:187], v[220:223]\n" \
    "ds_bpermute_b32 v173, v43, v161 offset:64\n" \
    "ds_bpermute_b32 v174, v43, v161 offset:128\n" \
    "v_mfma_f32_16x16x32_bf16 v[224:227], v[32:35], v[184:187], v[224:227]\n" \
    "ds_bpermute_b32 v175, v43, v161 offset:192\n" \
    "v_pk_mul_f16 v93, v93, v164 op_sel_hi:[1,0]\n" \
    "v_pk_mul_f16 v94, v94, v164 op_sel_hi:[1,0]\n" \
    "v_pk_mul_f16 v97, v97, v165 op_sel_hi:[1,0]\n" \
    "v_pk_mul_f16 v98, v98, v165 op_sel_hi:[1,0]\n" \
    "v_pk_mul_f16 v99, v99, v165 op_sel_hi:[1,0]\n" \
    "s_waitcnt lgkmcnt(8)\n" \
    "v_pk_mul_f16 v100, v100, v166 op_sel_hi:[1,0]\n" \
    "v_pk_mul_f16 v101, v101, v166 op_sel_hi:[1,0]\n" \
    "v_pk_mul_f16 v102, v102, v166 op_sel_hi:[1,0]\n" \
    "v_pk_mul_f16 v105, v105, v167 op_sel_hi:[1,0]\n" \
    "v_pk_mul_f16 v106, v106, v167 op_sel_hi:[1,0]\n" \
    "v_pk_mul_f16 v107, v107, v167 op_sel_hi:[1,0]\n" \
    "v_min3_u32 v142, v140, v144, v148\n" \
    "s_waitcnt lgkmcnt(4)\n" \
    "v_pk_mul_f16 v108, v108, v168 op_sel_hi:[1,0]\n" \
    "v_max3_u32 v143, v140, v144, v148\n" \
    "v_pk_mul_f16 v109, v109, v168 op_sel_hi:[1,0]\n" \
    "v_min3_u32 v142, v142, v152, v156\n" \
    "v_pk_mul_f16 v110, v110, v168 op_sel_hi:[1,0]\n" \
    "v_max3_u32 v143, v143, v152, v156\n" \
    "v_pk_mul_f16 v113, v113, v169 op_sel_hi:[1,0]\n" \
    "v_min3_u32 v142, v142, v160, s67\n" \
    "v_pk_mul_f16 v114, v114, v169 op_sel_hi:[1,0]\n" \
    "v_max3_u32 v143, v143, v160, s68\n" \
    "v_pk_mul_f16 v115, v115, v169 op_sel_hi:[1,0]\n" \
    "v_sub_u32 v142, v143, v142\n" \
    "v_pk_mul_f16 v116, v116, v170 op_sel_hi:[1,0]\n" \
    "v_cmp_ne_u32 vcc, 0xb800000, v142\n" \
    "v_pk_mul_f16 v117, v117, v170 op_sel_hi:[1,0]\n" \
    "s_cbranch_vccnz Lexit%=\n" \
    "v_pk_mul_f16 v118, v118, v170 op_sel_hi:[1,0]\n" \
    "v_pk_mul_f16 v121, v121, v171 op_sel_hi:[1,0]\n" \
    "v_pk_mul_f16 v122, v122, v171 op_sel_hi:[1,0]\n" \
    "v_cvt_pk_bf16_f32 v140, v204, v205\n" \
    "v_pk_mul_f16 v123, v123, v171 op_sel_hi:[1,0]\n" \
    "v_cvt_pk_bf16_f32 v141, v206, v207\n" \
    "v_mfma_f32_16x16x32_bf16 v[204:207], v[0:3], v[228:231], 0\n" \
    "s_waitcnt lgkmcnt(0)\n" \
    "v_pk_mul_f16 v124, v124, v172 op_sel_hi:[1,0]\n" \
    "v_mfma_f32_16x16x32_bf16 v[204:207], v[4:7], v[232:235], v[204:207]\n" \
    "v_cvt_pk_bf16_f32 v142, v208, v209\n" \
    "v_pk_mul_f16 v125, v125, v172 op_sel_hi:[1,0]\n" \
    "v_mfma_f32_16x16x32_bf16 v[204:207], v[8:11], v[236:239], v[204:207]\n" \
    "v_cvt_pk_bf16_f32 v143, v210, v211\n" \
    "v_pk_mul_f16 v126, v126, v172 op_sel_hi:[1,0]\n" \
    "v_mfma_f32_16x16x32_bf16 v[208:211], v[12:15], v[228:231], 0\n" \
    "v_cvt_pk_bf16_f32 v144, v212, v213\n" \
    "v_pk_mul_f16 v129, v129, v173 op_sel_hi:[1,0]\n" \
    "v_mfma_f32_16x16x32_bf16 v[208:211], v[16:19], v[232:235], v[208:211]\n" \
    "v_cvt_pk_bf16_f32 v145, v214, v215\n" \
    "v_pk_mul_f16 v130, v130, v173 op_sel_hi:[1,0]\n" \
    "v_mfma_f32_16x16x32_bf16 v[212:215], v[24:27], v[228:231], 0\n" \
    "ds_read_b128 v[228:231], v39 offset:49920\n" \
    "v_pk_mul_f16 v131, v131, v173 op_sel_hi:[1,0]\n" \
    "v_mfma_f32_16x16x32_bf16 v[212:215], v[28:31], v[232:235], v[212:215]\n" \
    "ds_read_b128 v[232:235], v39 offset:50960\n" \
    "v_pk_mul_f16 v132, v132, v174 op_sel_hi:[1,0]\n" \
    "v_mfma_f32_16x16x32_bf16 v[208:211], v[20:23], v[236:239], v[208:211]\n" \
    "v_pk_mul_f16 v133, v133, v174 op_sel_hi:[1,0]\n" \
    "v_pk_mul_f16 v134, v134, v174 op_sel_hi:[1,0]\n" \
    "v_mfma_f32_16x16x32_bf16 v[212:215], v[32:35], v[236:239], v[212:215]\n" \
    "ds_read_b128 v[236:239], v39 offset:52000\n" \
    "v_pk_mul_f16 v137, v137, v175 op_sel_hi:[1,0]\n" \
    "v_pk_mul_f16 v138, v138, v175 op_sel_hi:[1,0]\n" \
    "v_pk_mul_f16 v139, v139, v175 op_sel_hi:[1,0]\n" \
    "v_cvt_pk_bf16_f32 v216, v216, v217\n" \
    "v_cndmask_b32 v148, v55, v216, s[58:59]\n" \
    "v_cndmask_b32 v164, v216, v55, s[58:59]\n" \
    "v_cvt_pk_bf16_f32 v218, v218, v219\n" \
    "v_cndmask_b32 v149, v55, v218, s[58:59]\n" \
    "v_cndmask_b32 v165, v218, v55, s[58:59]\n" \
    "v_mfma_f32_16x16x32_bf16 v[216:219], v[0:3], v[240:243], 0\n" \
    "v_cvt_pk_bf16_f32 v220, v220, v221\n" \
    "v_cndmask_b32 v150, v55, v220, s[58:59]\n" \
    "v_mfma_f32_16x16x32_bf16 v[216:219], v[4:7], v[244:247], v[216:219]\n" \
    "v_cndmask_b32 v166, v220, v55, s[58:59]\n" \
    "v_cvt_pk_bf16_f32 v222, v222, v223\n" \
    "v_mfma_f32_16x16x32_bf16 v[216:219], v[8:11], v[248:251], v[216:219]\n" \
    "v_cndmask_b32 v151, v55, v222, s[58:59]\n" \
    "v_cndmask_b32 v167, v222, v55, s[58:59]\n" \
    "v_mfma_f32_16x16x32_bf16 v[220:223], v[12:15], v[240:243], 0\n" \
    "v_cvt_pk_bf16_f32 v224, v224, v225\n" \
    "v_cndmask_b32 v152, v55, v224, s[58:59]\n" \
    "v_mfma_f32_16x16x32_bf16 v[220:223], v[16:19], v[244:247], v[220:223]\n" \
    "v_cndmask_b32 v168, v224, v55, s[58:59]\n" \
    "v_cvt_pk_bf16_f32 v226, v226, v227\n" \
    "v_mfma_f32_16x16x32_bf16 v[220:223], v[20:23], v[248:251], v[220:223]\n" \
    "v_cndmask_b32 v153, v55, v226, s[58:59]\n" \
    "v_cndmask_b32 v169, v226, v55, s[58:59]\n" \
    "v_mfma_f32_16x16x32_bf16 v[224:227], v[24:27], v[240:243], 0\n" \
    "ds_read_b128 v[240:243], v39 offset:53040\n" \
    "s_cmp_eq_u32 s55, 0\n" \
    "s_cbranch_scc1 Lmw%=\n" \
    "s_waitcnt vmcnt(0)\n" \
    "Lmw%=:\n" \
    "v_mfma_f32_16x16x32_bf16 v[224:227], v[28:31], v[244:247], v[224:227]\n" \
    "ds_read_b128 v[244:247], v39 offset:54080\n" \
    "v_or3_b32 v255, v255, v188, v189\n" \
    "v_mfma_f32_16x16x32_bf16 v[224:227], v[32:35], v[248:251], v[224:227]\n" \
    "ds_read_b128 v[248:251], v39 offset:55120\n" \
    "v_or3_b32 v255, v255, v190, v191\n" \
    "v_or3_b32 v255, v255, v192, v193\n" \
    "v_cvt_pk_bf16_f32 v156, v204, v205\n" \
    "v_or3_b32 v255, v255, v194, v195\n" \
    "v_cvt_pk_bf16_f32 v157, v206, v207\n" \
    "s_waitcnt lgkmcnt(3)\n" \
    "v_mfma_f32_16x16x32_bf16 v[204:207], v[0:3], v[228:231], 0\n" \
    "v_or3_b32 v255, v255, v196, v197\n" \
    "v_cvt_pk_bf16_f32 v158, v208, v209\n" \
    "v_mfma_f32_16x16x32_bf16 v[204:207], v[4:7], v[232:235], v[204:207]\n" \
    "v_or3_b32 v255, v255, v198, v199\n" \
    "v_cvt_pk_bf16_f32 v159, v210, v211\n" \
    "v_mfma_f32_16x16x32_bf16 v[208:211], v[12:15], v[228:231], 0\n" \
    "v_or3_b32 v255, v255, v200, v201\n" \
    "v_cvt_pk_bf16_f32 v160, v212, v213\n" \
    "v_mfma_f32_16x16x32_bf16 v[208:211], v[16:19], v[232:235], v[208:211]\n" \
    "v_or3_b32 v255, v255, v202, v203\n" \
    "v_cvt_pk_bf16_f32 v161, v214, v215\n" \
    "v_mfma_f32_16x16x32_bf16 v[212:215], v[24:27], v[228:231], 0\n" \
    "v_or3_b32 v255, v255, v252, v253\n" \
    "v_or_b32 v255, v255, v254\n" \
    "v_mfma_f32_16x16x32_bf16 v[212:215], v[28:31], v[232:235], v[212:215]\n" \
    "v_and_b32 v255, s69, v255\n" \
    "v_cmp_ne_u32 vcc, 0, v255\n" \
    "v_mfma_f32_16x16x32_bf16 v[204:207], v[8:11], v[236:239], v[204:207]\n" \
    "s_cbranch_vccnz Lalt%=\n" \
    "v_cvt_pk_bf16_f32 v172, v216, v217\n" \
    "v_mfma_f32_16x16x32_bf16 v[208:211], v[20:23], v[236:239], v[208:211]\n" \
    "v_cvt_pk_bf16_f32 v173, v218, v219\n" \
    "v_cvt_pk_bf16_f32 v174, v220, v221\n" \
    "v_mfma_f32_16x16x32_bf16 v[212:215], v[32:35], v[236:239], v[212:215]\n" \
    "v_cvt_pk_bf16_f32 v175, v222, v223\n" \
    "v_cvt_pk_bf16_f32 v176, v224, v225\n" \
    "s_waitcnt lgkmcnt(0)\n" \
    "v_mfma_f32_16x16x32_bf16 v[216:219], v[0:3], v[240:243], 0\n" \
    "s_cmp_lg_u32 s55, 0\n" \
    "s_cbranch_scc1 Lh1%=\n" \
    "Lr1%=:\n" \
    "v_mfma_f32_16x16x32_bf16 v[220:223], v[12:15], v[240:243], 0\n" \
    "ds_read_b128 v[236:239], v44\n" \
    "ds_read_b128 v[228:231], v45 offset:256\n" \
    "v_mfma_f32_16x16x32_bf16 v[216:219], v[4:7], v[244:247], v[216:219]\n" \
    "ds_read_b128 v[232:235], v46 offset:256\n" \
    "ds_read_b128 v[252:255], v47 offset:256\n" \
    "v_mfma_f32_16x16x32_bf16 v[220:223], v[16:19], v[244:247], v[220:223]\n" \
    "ds_read_b128 v[0:3], v42\n" \
    "ds_read_b128 v[4:7], v42 offset:64\n" \
    "v_mfma_f32_16x16x32_bf16 v[216:219], v[8:11], v[248:251], v[216:219]\n" \
    "ds_read_b128 v[8:11], v42 offset:128\n" \
    "ds_read_b128 v[12:15], v41\n" \
    "v_mfma_f32_16x16x32_bf16 v[220:223], v[20:23], v[248:251], v[220:223]\n" \
    "ds_read_b128 v[16:19], v41 offset:1024\n" \
    "ds_read_b128 v[20:23], v41 offset:2048\n" \
    "v_cvt_pk_bf16_f32 v177, v226, v227\n" \
    "v_mfma_f32_16x16x32_bf16 v[224:227], v[24:27], v[240:243], 0\n" \
    "ds_read_b128 v[240:243], v45\n" \
    "ds_read_b128 v[24:27], v41 offset:3072\n" \
    "v_mfma_f32_16x16x32_bf16 v[224:227], v[28:31], v[244:247], v[224:227]\n" \
    "ds_read_b128 v[244:247], v46\n" \
    "v_cvt_pk_bf16_f32 v204, v204, v205\n" \
    "v_mfma_f32_16x16x32_bf16 v[224:227], v[32:35], v[248:251], v[224:227]\n" \
    "v_cndmask_b32 v180, v55, v204, s[58:59]\n" \
    "v_cndmask_b32 v196, v204, v55, s[58:59]\n" \
    "v_cvt_pk_bf16_f32 v206, v206, v207\n" \
    "v_cndmask_b32 v181, v55, v206, s[58:59]\n" \
    "v_cndmask_b32 v197, v206, v55, s[58:59]\n" \
    "v_cvt_pk_bf16_f32 v208, v208, v209\n" \
    "v_cndmask_b32 v182, v55, v208, s[58:59]\n" \
    "v_cndmask_b32 v198, v208, v55, s[58:59]\n" \
    "v_cvt_pk_bf16_f32 v210, v210, v211\n" \
    "v_cndmask_b32 v183, v55, v210, s[58:59]\n" \
    "v_cndmask_b32 v199, v210, v55, s[58:59]\n" \
    "v_cvt_pk_bf16_f32 v212, v212, v213\n" \
    "s_waitcnt lgkmcnt(5)\n" \
    "v_mfma_f32_16x16x32_f16 v[12:15], v[56:59], v[92:95], v[12:15]\n" \
    "ds_read_b128 v[248:251], v47\n" \
    "ds_read_b128 v[204:207], v41 offset:36992\n" \
    "s_waitcnt lgkmcnt(5)\n" \
    "v_mfma_f32_16x16x32_f16 v[16:19], v[68:71], v[92:95], v[16:19]\n" \
    "ds_read_b128 v[208:211], v41 offset:38016\n" \
    "ds_read_b128 v[28:31], v41 offset:4096\n" \
    "v_mfma_f32_16x16x32_f16 v[20:23], v[80:83], v[92:95], v[20:23]\n" \
    "ds_read_b128 v[32:35], v41 offset:5120\n" \
    "v_cndmask_b32 v184, v55, v212, s[58:59]\n" \
    "s_waitcnt lgkmcnt(5)\n" \
    "v_mfma_f32_16x16x32_f16 v[24:27], v[56:59], v[108:111], v[24:27]\n" \
    "v_cndmask_b32 v200, v212, v55, s[58:59]\n" \
    "v_cvt_pk_bf16_f32 v214, v214, v215\n" \
    "v_cndmask_b32 v185, v55, v214, s[58:59]\n" \
    "v_cndmask_b32 v201, v214, v55, s[58:59]\n" \
    "v_mov_b32 v55, 0x30000\n" \
    "v_cndmask_b32 v146, v55, v54, s[76:77]\n" \
    "v_cvt_pk_bf16_f32 v188, v216, v217\n" \
    "v_cvt_pk_bf16_f32 v189, v218, v219\n" \
    "v_cndmask_b32 v162, v55, v54, s[78:79]\n" \
    "v_cvt_pk_bf16_f32 v190, v220, v221\n" \
    "v_cvt_pk_bf16_f32 v191, v222, v223\n" \
    "v_cvt_pk_bf16_f32 v192, v224, v225\n" \
    "v_cndmask_b32 v178, v55, v54, s[80:81]\n" \
    "v_cvt_pk_bf16_f32 v193, v226, v227\n" \
    "v_cndmask_b32 v194, v55, v54, s[82:83]\n" \
    "s_waitcnt lgkmcnt(3)\n" \
    "v_mfma_f32_16x16x32_f16 v[204:207], v[236:239], v[92:95], v[204:207]\n" \
    "ds_read_b128 v[224:227], v44 offset:256\n" \
    "ds_read_b64 v[154:155], v146 offset:1152\n" \
    "s_waitcnt lgkmcnt(3)\n" \
    "v_mfma_f32_16x16x32_f16 v[208:211], v[236:239], v[108:111], v[208:211]\n" \
    "ds_read_b64 v[146:147], v146 offset:1024\n" \
    "ds_read_b64 v[170:171], v162 offset:1408\n" \
    "v_mfma_f32_16x16x32_f16 v[204:207], v[240:243], v[96:99], v[204:207]\n" \
    "ds_read_b64 v[162:163], v162 offset:1280\n" \
    "ds_read_b64 v[186:187], v178 offset:1664\n" \
    "v_mfma_f32_16x16x32_f16 v[208:211], v[240:243], v[112:115], v[208:211]\n" \
    "ds_read_b64 v[178:179], v178 offset:1536\n" \
    "ds_read_b128 v[212:215], v41 offset:39040\n" \
    "v_mfma_f32_16x16x32_f16 v[204:207], v[244:247], v[100:103], v[204:207]\n" \
    "ds_read_b128 v[216:219], v42 offset:192\n" \
    "ds_read_b64 v[202:203], v194 offset:1920\n" \
    "v_mfma_f32_16x16x32_f16 v[208:211], v[244:247], v[116:119], v[208:211]\n" \
    "ds_read_b64 v[194:195], v194 offset:1792\n" \
    "v_exp_f32 v12, v12\n" \
    "v_mfma_f32_16x16x32_f16 v[204:207], v[248:251], v[104:107], v[204:207]\n" \
    "v_exp_f32 v13, v13\n" \
    "s_waitcnt lgkmcnt(10)\n" \
    "v_mfma_f32_16x16x32_f16 v[0:3], v[56:59], v[224:227], v[0:3]\n" \
    "v_cvt_pk_bf16_f32 v12, v12, v13\n" \
    "v_exp_f32 v14, v14\n" \
    "v_mfma_f32_16x16x32_f16 v[4:7], v[68:71], v[224:227], v[4:7]\n" \
    "v_exp_f32 v15, v15\n" \
    "v_mfma_f32_16x16x32_f16 v[8:11], v[80:83], v[224:227], v[8:11]\n" \
    "ds_read_b32 v207, v55\n" \
    "v_exp_f32 v204, v204\n" \
    "s_waitcnt lgkmcnt(4)\n" \
    "v_mfma_f32_16x16x32_f16 v[212:215], v[236:239], v[124:127], v[212:215]\n" \
    "v_cvt_pk_bf16_f32 v206, v204, 0\n" \
    "v_cvt_pk_bf16_f32 v13, v14, v15\n" \
    "s_waitcnt lgkmcnt(2)\n" \
    "v_mfma_f32_16x16x32_f16 v[216:219], v[236:239], v[224:227], v[216:219]\n" \
    "v_exp_f32 v16, v16\n" \
    "v_mfma_f32_16x16x32_f16 v[212:215], v[240:243], v[128:131], v[212:215]\n" \
    "v_exp_f32 v17, v17\n" \
    "v_mfma_f32_16x16x32_f16 v[216:219], v[240:243], v[228:231], v[216:219]\n" \
    "v_cvt_pk_bf16_f32 v14, v16, v17\n" \
    "v_exp_f32 v18, v18\n" \
    "v_mfma_f32_16x16x32_f16 v[0:3], v[58:61], v[228:231], v[0:3]\n" \
    "v_exp_f32 v19, v19\n" \
    "v_mfma_f32_16x16x32_f16 v[4:7], v[70:73], v[228:231], v[4:7]\n" \
    "v_cvt_pk_bf16_f32 v15, v18, v19\n" \
    "v_exp_f32 v20, v20\n" \
    "v_mfma_f32_16x16x32_f16 v[8:11], v[82:85], v[228:231], v[8:11]\n" \
    "ds_read_b128 v[16:19], v41 offset:10272\n" \
    "v_exp_f32 v21, v21\n" \
    "v_mfma_f32_16x16x32_f16 v[212:215], v[244:247], v[132:135], v[212:215]\n" \
    "v_cvt_pk_bf16_f32 v204, v20, v21\n" \
    "v_exp_f32 v22, v22\n" \
    "v_mfma_f32_16x16x32_f16 v[216:219], v[244:247], v[232:235], v[216:219]\n" \
    "v_exp_f32 v23, v23\n" \
    "v_mfma_f32_16x16x32_f16 v[0:3], v[62:65], v[232:235], v[0:3]\n" \
    "v_cvt_pk_bf16_f32 v205, v22, v23\n" \
    "v_exp_f32 v24, v24\n" \
    "v_mfma_f32_16x16x32_f16 v[4:7], v[74:77], v[232:235], v[4:7]\n" \
    "ds_read_b128 v[20:23], v41 offset:11296\n" \
    "v_exp_f32 v25, v25\n" \
    "v_mfma_f32_16x16x32_f16 v[8:11], v[86:89], v[232:235], v[8:11]\n" \
    "v_cvt_pk_bf16_f32 v24, v24, v25\n" \
    "v_exp_f32 v26, v26\n" \
    "v_mfma_f32_16x16x32_f16 v[208:211], v[248:251], v[120:123], v[208:211]\n" \
    "v_exp_f32 v27, v27\n" \
    "v_mfma_f32_16x16x32_f16 v[212:215], v[248:251], v[136:139], v[212:215]\n" \
    "v_cvt_pk_bf16_f32 v25, v26, v27\n" \
    "v_mfma_f32_16x16x32_f16 v[216:219], v[248:251], v[252:255], v[216:219]\n" \
    "v_mfma_f32_16x16x32_f16 v[0:3], v[64:67], v[252:255], v[0:3]\n" \
    "v_mfma_f32_16x16x32_f16 v[4:7], v[76:79], v[252:255], v[4:7]\n" \
    "v_mfma_f32_16x16x32_f16 v[8:11], v[88:91], v[252:255], v[8:11]\n" \
    "s_nop 0\n" \
    "v_exp_f32 v208, v208\n" \
    "v_mfma_f32_16x16x32_f16 v[28:31], v[68:71], v[108:111], v[28:31]\n" \
    "ds_read_b32 v211, v55\n" \
    "ds_read_b32 v215, v55\n" \
    "v_mfma_f32_16x16x32_f16 v[32:35], v[80:83], v[108:111], v[32:35]\n" \
    "ds_read_b32 v219, v55\n" \
    "v_cvt_pk_bf16_f32 v210, v208, 0\n" \
    "v_mfma_f32_16x16x32_bf16 v[240:243], v[148:151], v[12:15], 0\n" \
    "v_exp_f32 v212, v212\n" \
    "v_mfma_f32_16x16x32_bf16 v[236:239], v[140:143], v[12:15], 0\n" \
    "ds_read_b128 v[12:15], v41 offset:9248\n" \
    "v_cvt_pk_bf16_f32 v214, v212, 0\n" \
    "s_waitcnt lgkmcnt(4)\n" \
    "v_mfma_f32_16x16x32_bf16 v[240:243], v[152:155], v[204:207], v[240:243]\n" \
    "v_exp_f32 v216, v216\n" \
    "v_mfma_f32_16x16x32_bf16 v[236:239], v[144:147], v[204:207], v[236:239]\n" \
    "v_cvt_pk_bf16_f32 v218, v216, 0\n" \
    "v_exp_f32 v0, v0\n" \
    "v_mfma_f32_16x16x32_f16 v[16:19], v[70:73], v[96:99], v[16:19]\n" \
    "v_exp_f32 v1, v1\n" \
    "v_mfma_f32_16x16x32_f16 v[20:23], v[82:85], v[96:99], v[20:23]\n" \
    "v_cvt_pk_bf16_f32 v220, v0, v1\n" \
    "v_exp_f32 v2, v2\n" \
    "v_exp_f32 v3, v3\n" \
    "v_exp_f32 v4, v4\n" \
    "v_cvt_pk_bf16_f32 v221, v2, v3\n" \
    "v_exp_f32 v5, v5\n" \
    "s_waitcnt lgkmcnt(0)\n" \
    "v_mfma_f32_16x16x32_f16 v[12:15], v[58:61], v[96:99], v[12:15]\n" \
    "ds_read_b128 v[0:3], v41 offset:6144\n" \
    "v_cvt_pk_bf16_f32 v222, v4, v5\n" \
    "v_exp_f32 v6, v6\n" \
    "v_exp_f32 v7, v7\n" \
    "v_exp_f32 v8, v8\n" \
    "v_cvt_pk_bf16_f32 v223, v6, v7\n" \
    "v_exp_f32 v9, v9\n" \
    "v_exp_f32 v10, v10\n" \
    "v_mfma_f32_16x16x32_bf16 v[248:251], v[148:151], v[220:223], 0\n" \
    "ds_read_b128 v[4:7], v41 offset:7168\n" \
    "ds_read_b128 v[252:255], v41 offset:23616\n" \
    "v_mfma_f32_16x16x32_bf16 v[244:247], v[140:143], v[220:223], 0\n" \
    "v_cvt_pk_bf16_f32 v216, v8, v9\n" \
    "v_exp_f32 v11, v11\n" \
    "v_rcp_f32 v242, v242\n" \
    "v_cvt_pk_bf16_f32 v217, v10, v11\n" \
    "v_mul_f32 v236, v236, v242\n" \
    "v_mul_f32 v237, v237, v242\n" \
    "v_mfma_f32_16x16x32_bf16 v[248:251], v[152:155], v[216:219], v[248:251]\n" \
    "ds_read_b128 v[8:11], v41 offset:8192\n" \
    "v_cvt_pk_bf16_f32 v224, v236, v237\n" \
    "v_mfma_f32_16x16x32_bf16 v[244:247], v[144:147], v[216:219], v[244:247]\n" \
    "v_mul_f32 v238, v238, v242\n" \
    "v_mul_f32 v239, v239, v242\n" \
    "s_waitcnt lgkmcnt(3)\n" \
    "v_mfma_f32_16x16x32_f16 v[0:3], v[56:59], v[124:127], v[0:3]\n" \
    "v_cvt_pk_bf16_f32 v225, v238, v239\n" \
    "v_mul_f32 v240, v240, v242\n" \
    "v_mul_f32 v241, v241, v242\n" \
    "v_cvt_pk_bf16_f32 v226, v240, v241\n" \
    "v_exp_f32 v28, v28\n" \
    "s_waitcnt lgkmcnt(1)\n" \
    "v_mfma_f32_16x16x32_f16 v[4:7], v[68:71], v[124:127], v[4:7]\n" \
    "ds_write_b128 v44, v[244:247] offset:512\n" \
    "v_mfma_f32_16x16x32_f16 v[252:255], v[86:89], v[116:119], v[252:255]\n" \
    "ds_write_b128 v44, v[248:251] offset:768\n" \
    "s_waitcnt lgkmcnt(2)\n" \
    "v_mfma_f32_16x16x32_f16 v[8:11], v[80:83], v[124:127], v[8:11]\n" \
    "v_exp_f32 v29, v29\n" \
    "v_exp_f32 v30, v30\n" \
    "v_cvt_pk_bf16_f32 v26, v28, v29\n" \
    "v_exp_f32 v31, v31\n" \
    "v_exp_f32 v32, v32\n" \
    "v_cvt_pk_bf16_f32 v27, v30, v31\n" \
    "v_exp_f32 v33, v33\n" \
    "v_exp_f32 v34, v34\n" \
    "v_mfma_f32_16x16x32_bf16 v[240:243], v[148:151], v[24:27], 0\n" \
    "ds_read_b128 v[28:31], v41 offset:13344\n" \
    "v_cvt_pk_bf16_f32 v208, v32, v33\n" \
    "v_mfma_f32_16x16x32_bf16 v[236:239], v[140:143], v[24:27], 0\n" \
    "ds_read_b128 v[24:27], v41 offset:12320\n" \
    "v_exp_f32 v35, v35\n" \
    "v_exp_f32 v0, v0\n" \
    "v_cvt_pk_bf16_f32 v209, v34, v35\n" \
    "v_exp_f32 v1, v1\n" \
    "v_exp_f32 v2, v2\n" \
    "v_mfma_f32_16x16x32_bf16 v[240:243], v[152:155], v[208:211], v[240:243]\n" \
    "ds_read_b128 v[32:35], v41 offset:14368\n" \
    "v_cvt_pk_bf16_f32 v0, v0, v1\n" \
    "v_mfma_f32_16x16x32_bf16 v[236:239], v[144:147], v[208:211], v[236:239]\n" \
    "v_exp_f32 v3, v3\n" \
    "v_exp_f32 v4, v4\n" \
    "v_cvt_pk_bf16_f32 v1, v2, v3\n" \
    "v_exp_f32 v5, v5\n" \
    "v_exp_f32 v6, v6\n" \
    "s_waitcnt lgkmcnt(2)\n" \
    "v_mfma_f32_16x16x32_f16 v[28:31], v[70:73], v[112:115], v[28:31]\n" \
    "v_rcp_f32 v242, v242\n" \
    "s_waitcnt lgkmcnt(1)\n" \
    "v_mfma_f32_16x16x32_f16 v[24:27], v[58:61], v[112:115], v[24:27]\n" \
    "v_mul_f32 v236, v236, v242\n" \
    "v_mul_f32 v237, v237, v242\n" \
    "v_cvt_pk_bf16_f32 v228, v236, v237\n" \
    "v_mul_f32 v238, v238, v242\n" \
    "v_mul_f32 v239, v239, v242\n" \
    "v_cvt_pk_bf16_f32 v229, v238, v239\n" \
    "v_mul_f32 v240, v240, v242\n" \
    "s_waitcnt lgkmcnt(0)\n" \
    "v_mfma_f32_16x16x32_f16 v[32:35], v[82:85], v[112:115], v[32:35]\n" \
    "v_mul_f32 v241, v241, v242\n" \
    "v_cvt_pk_bf16_f32 v230, v240, v241\n" \
    "v_cvt_pk_bf16_f32 v2, v4, v5\n" \
    "v_exp_f32 v7, v7\n" \
    "v_exp_f32 v8, v8\n" \
    "v_cvt_pk_bf16_f32 v3, v6, v7\n" \
    "v_exp_f32 v9, v9\n" \
    "v_exp_f32 v10, v10\n" \
    "v_mfma_f32_16x16x32_bf16 v[248:251], v[148:151], v[0:3], 0\n" \
    "ds_read_b128 v[4:7], v41 offset:16416\n" \
    "v_cvt_pk_bf16_f32 v212, v8, v9\n" \
    "v_mfma_f32_16x16x32_bf16 v[244:247], v[140:143], v[0:3], 0\n" \
    "ds_read_b128 v[0:3], v41 offset:15392\n" \
    "v_exp_f32 v11, v11\n" \
    "v_exp_f32 v12, v12\n" \
    "v_cvt_pk_bf16_f32 v213, v10, v11\n" \
    "v_exp_f32 v13, v13\n" \
    "v_exp_f32 v14, v14\n" \
    "v_mfma_f32_16x16x32_bf16 v[248:251], v[152:155], v[212:215], v[248:251]\n" \
    "ds_read_b128 v[8:11], v41 offset:17440\n" \
    "ds_read_b128 v[152:155], v41 offset:21568\n" \
    "v_mfma_f32_16x16x32_bf16 v[244:247], v[144:147], v[212:215], v[244:247]\n" \
    "v_cvt_pk_bf16_f32 v12, v12, v13\n" \
    "v_exp_f32 v15, v15\n" \
    "v_exp_f32 v16, v16\n" \
    "v_cvt_pk_bf16_f32 v13, v14, v15\n" \
    "v_exp_f32 v17, v17\n" \
    "s_waitcnt lgkmcnt(3)\n" \
    "v_mfma_f32_16x16x32_f16 v[4:7], v[70:73], v[128:131], v[4:7]\n" \
    "ds_read_b128 v[68:71], v41 offset:19520\n" \
    "v_rcp_f32 v250, v250\n" \
    "s_waitcnt lgkmcnt(3)\n" \
    "v_mfma_f32_16x16x32_f16 v[0:3], v[58:61], v[128:131], v[0:3]\n" \
    "ds_read_b128 v[56:59], v41 offset:18496\n" \
    "v_mul_f32 v244, v244, v250\n" \
    "v_mul_f32 v245, v245, v250\n" \
    "v_cvt_pk_bf16_f32 v232, v244, v245\n" \
    "v_mul_f32 v246, v246, v250\n" \
    "v_mul_f32 v247, v247, v250\n" \
    "v_cvt_pk_bf16_f32 v233, v246, v247\n" \
    "v_mfma_f32_16x16x32_bf16 v[244:247], v[156:159], v[220:223], 0\n" \
    "v_mul_f32 v248, v248, v250\n" \
    "v_mul_f32 v249, v249, v250\n" \
    "v_mfma_f32_16x16x32_bf16 v[244:247], v[160:163], v[216:219], v[244:247]\n" \
    "v_cvt_pk_bf16_f32 v234, v248, v249\n" \
    "v_cvt_pk_bf16_f32 v14, v16, v17\n" \
    "v_mfma_f32_16x16x32_bf16 v[248:251], v[164:167], v[220:223], 0\n" \
    "v_exp_f32 v18, v18\n" \
    "v_mfma_f32_16x16x32_bf16 v[248:251], v[168:171], v[216:219], v[248:251]\n" \
    "v_exp_f32 v19, v19\n" \
    "s_waitcnt lgkmcnt(1)\n" \
    "v_mfma_f32_16x16x32_f16 v[8:11], v[82:85], v[128:131], v[8:11]\n" \
    "ds_write_b128 v45, v[244:247] offset:512\n" \
    "s_waitcnt lgkmcnt(1)\n" \
    "v_mfma_f32_16x16x32_f16 v[56:59], v[62:65], v[100:103], v[56:59]\n" \
    "ds_read_b128 v[80:83], v41 offset:20544\n" \
    "v_cvt_pk_bf16_f32 v15, v18, v19\n" \
    "v_mfma_f32_16x16x32_f16 v[68:71], v[74:77], v[100:103], v[68:71]\n" \
    "ds_write_b128 v45, v[248:251] offset:768\n" \
    "v_mfma_f32_16x16x32_bf16 v[240:243], v[164:167], v[12:15], 0\n" \
    "v_exp_f32 v20, v20\n" \
    "v_mfma_f32_16x16x32_bf16 v[236:239], v[156:159], v[12:15], 0\n" \
    "v_exp_f32 v21, v21\n" \
    "v_mfma_f32_16x16x32_f16 v[152:155], v[62:65], v[116:119], v[152:155]\n" \
    "v_cvt_pk_bf16_f32 v204, v20, v21\n" \
    "v_exp_f32 v22, v22\n" \
    "v_exp_f32 v23, v23\n" \
    "v_exp_f32 v24, v24\n" \
    "s_waitcnt lgkmcnt(1)\n" \
    "v_mfma_f32_16x16x32_f16 v[80:83], v[86:89], v[100:103], v[80:83]\n" \
    "v_cvt_pk_bf16_f32 v205, v22, v23\n" \
    "v_exp_f32 v25, v25\n" \
    "v_exp_f32 v26, v26\n" \
    "v_mfma_f32_16x16x32_bf16 v[240:243], v[168:171], v[204:207], v[240:243]\n" \
    "v_cvt_pk_bf16_f32 v24, v24, v25\n" \
    "v_exp_f32 v27, v27\n" \
    "v_mfma_f32_16x16x32_bf16 v[236:239], v[160:163], v[204:207], v[236:239]\n" \
    "v_cvt_pk_bf16_f32 v25, v26, v27\n" \
    "v_exp_f32 v28, v28\n" \
    "v_exp_f32 v29, v29\n" \
    "v_exp_f32 v30, v30\n" \
    "v_cvt_pk_bf16_f32 v26, v28, v29\n" \
    "v_rcp_f32 v240, v240\n" \
    "v_exp_f32 v31, v31\n" \
    "v_mul_f32 v242, v242, v240\n" \
    "v_mul_f32 v243, v243, v240\n" \
    "v_cvt_pk_bf16_f32 v227, v242, v243\n" \
    "v_mul_f32 v236, v236, v240\n" \
    "v_mul_f32 v237, v237, v240\n" \
    "v_cvt_pk_bf16_f32 v140, v236, v237\n" \
    "v_mul_f32 v238, v238, v240\n" \
    "v_mul_f32 v239, v239, v240\n" \
    "v_cvt_pk_bf16_f32 v141, v238, v239\n" \
    "v_cvt_pk_bf16_f32 v27, v30, v31\n" \
    "v_exp_f32 v32, v32\n" \
    "v_exp_f32 v33, v33\n" \
    "v_mfma_f32_16x16x32_bf16 v[240:243], v[164:167], v[24:27], 0\n" \
    "v_cvt_pk_bf16_f32 v208, v32, v33\n" \
    "v_exp_f32 v34, v34\n" \
    "v_mfma_f32_16x16x32_bf16 v[236:239], v[156:159], v[24:27], 0\n" \
    "v_exp_f32 v35, v35\n" \
    "v_exp_f32 v0, v0\n" \
    "v_cvt_pk_bf16_f32 v209, v34, v35\n" \
    "v_exp_f32 v1, v1\n" \
    "v_exp_f32 v2, v2\n" \
    "v_mfma_f32_16x16x32_bf16 v[240:243], v[168:171], v[208:211], v[240:243]\n" \
    "v_cvt_pk_bf16_f32 v0, v0, v1\n" \
    "v_exp_f32 v3, v3\n" \
    "v_mfma_f32_16x16x32_bf16 v[236:239], v[160:163], v[208:211], v[236:239]\n" \
    "v_cvt_pk_bf16_f32 v1, v2, v3\n" \
    "v_exp_f32 v4, v4\n" \
    "v_exp_f32 v5, v5\n" \
    "v_exp_f32 v6, v6\n" \
    "v_cvt_pk_bf16_f32 v2, v4, v5\n" \
    "v_rcp_f32 v240, v240\n" \
    "v_exp_f32 v7, v7\n" \
    "v_mul_f32 v242, v242, v240\n" \
    "v_mul_f32 v243, v243, v240\n" \
    "v_cvt_pk_bf16_f32 v231, v242, v243\n" \
    "v_mul_f32 v236, v236, v240\n" \
    "v_mul_f32 v237, v237, v240\n" \
    "v_cvt_pk_bf16_f32 v144, v236, v237\n" \
    "v_mul_f32 v238, v238, v240\n" \
    "v_mul_f32 v239, v239, v240\n" \
    "v_cvt_pk_bf16_f32 v145, v238, v239\n" \
    "v_cvt_pk_bf16_f32 v3, v6, v7\n" \
    "v_exp_f32 v8, v8\n" \
    "v_exp_f32 v9, v9\n" \
    "v_mfma_f32_16x16x32_bf16 v[248:251], v[164:167], v[0:3], 0\n" \
    "v_cvt_pk_bf16_f32 v212, v8, v9\n" \
    "v_exp_f32 v10, v10\n" \
    "v_mfma_f32_16x16x32_bf16 v[244:247], v[156:159], v[0:3], 0\n" \
    "buffer_load_dwordx4 v[0:3], v36, s[40:43], s57 offen\n" \
    "buffer_load_dwordx4 v[12:15], v37, s[40:43], s57 offen\n" \
    "buffer_load_dwordx4 v[24:27], v38, s[40:43], s57 offen\n" \
    "v_exp_f32 v11, v11\n" \
    "v_exp_f32 v56, v56\n" \
    "v_cvt_pk_bf16_f32 v213, v10, v11\n" \
    "v_exp_f32 v57, v57\n" \
    "v_exp_f32 v58, v58\n" \
    "v_mfma_f32_16x16x32_bf16 v[248:251], v[168:171], v[212:215], v[248:251]\n" \
    "ds_read_b128 v[168:171], v41 offset:22592\n" \
    "buffer_load_dwordx4 v[4:7], v36, s[40:43], s57 offen offset:64\n" \
    "v_mfma_f32_16x16x32_bf16 v[244:247], v[160:163], v[212:215], v[244:247]\n" \
    "buffer_load_dwordx4 v[16:19], v37, s[40:43], s57 offen offset:64\n" \
    "buffer_load_dwordx4 v[28:31], v38, s[40:43], s57 offen offset:64\n" \
    "buffer_load_dwordx4 v[8:11], v36, s[40:43], s57 offen offset:128\n" \
    "v_cvt_pk_bf16_f32 v56, v56, v57\n" \
    "v_exp_f32 v59, v59\n" \
    "v_rcp_f32 v248, v248\n" \
    "v_cvt_pk_bf16_f32 v57, v58, v59\n" \
    "v_mul_f32 v250, v250, v248\n" \
    "v_mul_f32 v251, v251, v248\n" \
    "v_cvt_pk_bf16_f32 v235, v250, v251\n" \
    "v_mul_f32 v244, v244, v248\n" \
    "v_mul_f32 v245, v245, v248\n" \
    "v_cvt_pk_bf16_f32 v148, v244, v245\n" \
    "v_mul_f32 v246, v246, v248\n" \
    "v_mul_f32 v247, v247, v248\n" \
    "v_mfma_f32_16x16x32_bf16 v[248:251], v[180:183], v[220:223], 0\n" \
    "buffer_load_dwordx4 v[20:23], v37, s[40:43], s57 offen offset:128\n" \
    "s_waitcnt lgkmcnt(0)\n" \
    "v_mfma_f32_16x16x32_f16 v[168:171], v[74:77], v[116:119], v[168:171]\n" \
    "buffer_load_dwordx4 v[32:35], v38, s[40:43], s57 offen offset:128\n" \
    "v_mfma_f32_16x16x32_bf16 v[248:251], v[184:187], v[216:219], v[248:251]\n" \
    "v_cvt_pk_bf16_f32 v149, v246, v247\n" \
    "v_exp_f32 v68, v68\n" \
    "v_mfma_f32_16x16x32_bf16 v[244:247], v[172:175], v[220:223], 0\n" \
    "v_exp_f32 v69, v69\n" \
    "v_mfma_f32_16x16x32_bf16 v[244:247], v[176:179], v[216:219], v[244:247]\n" \
    "v_cvt_pk_bf16_f32 v58, v68, v69\n" \
    "v_exp_f32 v70, v70\n" \
    "v_exp_f32 v71, v71\n" \
    "v_exp_f32 v80, v80\n" \
    "v_cvt_pk_bf16_f32 v59, v70, v71\n" \
    "v_exp_f32 v81, v81\n" \
    "v_exp_f32 v82, v82\n" \
    "v_mfma_f32_16x16x32_bf16 v[240:243], v[180:183], v[56:59], 0\n" \
    "ds_read_b128 v[68:71], v41 offset:25664\n" \
    "ds_write_b128 v46, v[244:247] offset:512\n" \
    "v_mfma_f32_16x16x32_bf16 v[236:239], v[172:175], v[56:59], 0\n" \
    "ds_read_b128 v[56:59], v41 offset:24640\n" \
    "ds_write_b128 v46, v[248:251] offset:768\n" \
    "v_cvt_pk_bf16_f32 v204, v80, v81\n" \
    "v_exp_f32 v83, v83\n" \
    "v_exp_f32 v152, v152\n" \
    "v_cvt_pk_bf16_f32 v205, v82, v83\n" \
    "v_exp_f32 v153, v153\n" \
    "v_exp_f32 v154, v154\n" \
    "v_mfma_f32_16x16x32_bf16 v[240:243], v[184:187], v[204:207], v[240:243]\n" \
    "ds_read_b128 v[80:83], v41 offset:26688\n" \
    "v_cvt_pk_bf16_f32 v152, v152, v153\n" \
    "v_mfma_f32_16x16x32_bf16 v[236:239], v[176:179], v[204:207], v[236:239]\n" \
    "v_exp_f32 v155, v155\n" \
    "s_waitcnt lgkmcnt(3)\n" \
    "v_mfma_f32_16x16x32_f16 v[68:71], v[74:77], v[132:135], v[68:71]\n" \
    "v_cvt_pk_bf16_f32 v153, v154, v155\n" \
    "v_exp_f32 v168, v168\n" \
    "s_waitcnt lgkmcnt(1)\n" \
    "v_mfma_f32_16x16x32_f16 v[56:59], v[62:65], v[132:135], v[56:59]\n" \
    "v_rcp_f32 v242, v242\n" \
    "v_exp_f32 v169, v169\n" \
    "v_mul_f32 v236, v236, v242\n" \
    "v_mul_f32 v237, v237, v242\n" \
    "v_cvt_pk_bf16_f32 v142, v236, v237\n" \
    "v_mul_f32 v238, v238, v242\n" \
    "v_mul_f32 v239, v239, v242\n" \
    "v_cvt_pk_bf16_f32 v143, v238, v239\n" \
    "v_mul_f32 v240, v240, v242\n" \
    "v_mul_f32 v241, v241, v242\n" \
    "v_cvt_pk_bf16_f32 v156, v240, v241\n" \
    "s_waitcnt lgkmcnt(0)\n" \
    "v_mfma_f32_16x16x32_f16 v[80:83], v[86:89], v[132:135], v[80:83]\n" \
    "v_cvt_pk_bf16_f32 v154, v168, v169\n" \
    "v_exp_f32 v170, v170\n" \
    "v_exp_f32 v171, v171\n" \
    "v_exp_f32 v252, v252\n" \
    "v_cvt_pk_bf16_f32 v155, v170, v171\n" \
    "v_exp_f32 v253, v253\n" \
    "v_exp_f32 v254, v254\n" \
    "v_mfma_f32_16x16x32_bf16 v[240:243], v[180:183], v[152:155], 0\n" \
    "ds_read_b128 v[168:171], v41 offset:28768\n" \
    "v_cvt_pk_bf16_f32 v208, v252, v253\n" \
    "v_mfma_f32_16x16x32_bf16 v[236:239], v[172:175], v[152:155], 0\n" \
    "ds_read_b128 v[152:155], v41 offset:27744\n" \
    "v_exp_f32 v255, v255\n" \
    "v_exp_f32 v56, v56\n" \
    "v_cvt_pk_bf16_f32 v209, v254, v255\n" \
    "v_exp_f32 v57, v57\n" \
    "v_exp_f32 v58, v58\n" \
    "v_mfma_f32_16x16x32_bf16 v[240:243], v[184:187], v[208:211], v[240:243]\n" \
    "ds_read_b128 v[252:255], v41 offset:29792\n" \
    "v_cvt_pk_bf16_f32 v56, v56, v57\n" \
    "v_mfma_f32_16x16x32_bf16 v[236:239], v[176:179], v[208:211], v[236:239]\n" \
    "v_exp_f32 v59, v59\n" \
    "v_exp_f32 v68, v68\n" \
    "v_cvt_pk_bf16_f32 v57, v58, v59\n" \
    "v_exp_f32 v69, v69\n" \
    "v_exp_f32 v70, v70\n" \
    "s_waitcnt lgkmcnt(2)\n" \
    "v_mfma_f32_16x16x32_f16 v[168:171], v[76:79], v[104:107], v[168:171]\n" \
    "v_rcp_f32 v242, v242\n" \
    "s_waitcnt lgkmcnt(1)\n" \
    "v_mfma_f32_16x16x32_f16 v[152:155], v[64:67], v[104:107], v[152:155]\n" \
    "v_mul_f32 v236, v236, v242\n" \
    "v_mul_f32 v237, v237, v242\n" \
    "v_cvt_pk_bf16_f32 v146, v236, v237\n" \
    "v_mul_f32 v238, v238, v242\n" \
    "v_mul_f32 v239, v239, v242\n" \
    "v_cvt_pk_bf16_f32 v147, v238, v239\n" \
    "v_mul_f32 v240, v240, v242\n" \
    "s_waitcnt lgkmcnt(0)\n" \
    "v_mfma_f32_16x16x32_f16 v[252:255], v[88:91], v[104:107], v[252:255]\n" \
    "v_mul_f32 v241, v241, v242\n" \
    "v_cvt_pk_bf16_f32 v160, v240, v241\n" \
    "v_cvt_pk_bf16_f32 v58, v68, v69\n" \
    "v_exp_f32 v71, v71\n" \
    "v_exp_f32 v80, v80\n" \
    "v_cvt_pk_bf16_f32 v59, v70, v71\n" \
    "v_exp_f32 v81, v81\n" \
    "v_exp_f32 v82, v82\n" \
    "v_mfma_f32_16x16x32_bf16 v[248:251], v[180:183], v[56:59], 0\n" \
    "ds_read_b128 v[68:71], v41 offset:31840\n" \
    "v_cvt_pk_bf16_f32 v212, v80, v81\n" \
    "v_mfma_f32_16x16x32_bf16 v[244:247], v[172:175], v[56:59], 0\n" \
    "ds_read_b128 v[56:59], v41 offset:30816\n" \
    "v_exp_f32 v83, v83\n" \
    "v_exp_f32 v152, v152\n" \
    "v_cvt_pk_bf16_f32 v213, v82, v83\n" \
    "v_exp_f32 v153, v153\n" \
    "v_exp_f32 v154, v154\n" \
    "v_mfma_f32_16x16x32_bf16 v[248:251], v[184:187], v[212:215], v[248:251]\n" \
    "ds_read_b128 v[80:83], v41 offset:32864\n" \
    "v_cvt_pk_bf16_f32 v152, v152, v153\n" \
    "v_mfma_f32_16x16x32_bf16 v[244:247], v[176:179], v[212:215], v[244:247]\n" \
    "v_exp_f32 v155, v155\n" \
    "v_exp_f32 v168, v168\n" \
    "v_cvt_pk_bf16_f32 v153, v154, v155\n" \
    "v_exp_f32 v169, v169\n" \
    "v_exp_f32 v170, v170\n" \
    "s_waitcnt lgkmcnt(2)\n" \
    "v_mfma_f32_16x16x32_f16 v[68:71], v[76:79], v[120:123], v[68:71]\n" \
    "v_rcp_f32 v250, v250\n" \
    "s_waitcnt lgkmcnt(1)\n" \
    "v_mfma_f32_16x16x32_f16 v[56:59], v[64:67], v[120:123], v[56:59]\n" \
    "v_mul_f32 v244, v244, v250\n" \
    "v_mul_f32 v245, v245, v250\n" \
    "v_cvt_pk_bf16_f32 v150, v244, v245\n" \
    "v_mul_f32 v246, v246, v250\n" \
    "v_mul_f32 v247, v247, v250\n" \
    "v_cvt_pk_bf16_f32 v151, v246, v247\n" \
    "v_mfma_f32_16x16x32_bf16 v[244:247], v[188:191], v[220:223], 0\n" \
    "v_mul_f32 v248, v248, v250\n" \
    "v_mul_f32 v249, v249, v250\n" \
    "s_waitcnt lgkmcnt(0)\n" \
    "v_mfma_f32_16x16x32_f16 v[80:83], v[88:91], v[120:123], v[80:83]\n" \
    "v_cvt_pk_bf16_f32 v164, v248, v249\n" \
    "v_cvt_pk_bf16_f32 v154, v168, v169\n" \
    "v_mfma_f32_16x16x32_bf16 v[248:251], v[196:199], v[220:223], 0\n" \
    "v_exp_f32 v171, v171\n" \
    "v_mfma_f32_16x16x32_bf16 v[248:251], v[200:203], v[216:219], v[248:251]\n" \
    "v_cvt_pk_bf16_f32 v155, v170, v171\n" \
    "v_exp_f32 v252, v252\n" \
    "v_mfma_f32_16x16x32_bf16 v[244:247], v[192:195], v[216:219], v[244:247]\n" \
    "ds_read_b128 v[168:171], v41 offset:34912\n" \
    "v_exp_f32 v253, v253\n" \
    "v_mfma_f32_16x16x32_bf16 v[240:243], v[196:199], v[152:155], 0\n" \
    "v_cvt_pk_bf16_f32 v204, v252, v253\n" \
    "v_exp_f32 v254, v254\n" \
    "v_mfma_f32_16x16x32_bf16 v[236:239], v[188:191], v[152:155], 0\n" \
    "ds_read_b128 v[152:155], v41 offset:33888\n" \
    "v_exp_f32 v255, v255\n" \
    "v_exp_f32 v56, v56\n" \
    "v_cvt_pk_bf16_f32 v205, v254, v255\n" \
    "v_exp_f32 v57, v57\n" \
    "v_exp_f32 v58, v58\n" \
    "v_mfma_f32_16x16x32_bf16 v[240:243], v[200:203], v[204:207], v[240:243]\n" \
    "ds_read_b128 v[252:255], v41 offset:35936\n" \
    "ds_write_b128 v47, v[244:247] offset:512\n" \
    "v_mfma_f32_16x16x32_bf16 v[236:239], v[192:195], v[204:207], v[236:239]\n" \
    "ds_write_b128 v47, v[248:251] offset:768\n" \
    "s_waitcnt lgkmcnt(3)\n" \
    "v_mfma_f32_16x16x32_f16 v[152:155], v[64:67], v[136:139], v[152:155]\n" \
    "v_cvt_pk_bf16_f32 v56, v56, v57\n" \
    "v_exp_f32 v59, v59\n" \
    "v_mfma_f32_16x16x32_f16 v[168:171], v[76:79], v[136:139], v[168:171]\n" \
    "v_rcp_f32 v240, v240\n" \
    "v_cvt_pk_bf16_f32 v57, v58, v59\n" \
    "s_waitcnt lgkmcnt(2)\n" \
    "v_mfma_f32_16x16x32_f16 v[252:255], v[88:91], v[136:139], v[252:255]\n" \
    "v_mul_f32 v242, v242, v240\n" \
    "v_mul_f32 v243, v243, v240\n" \
    "v_cvt_pk_bf16_f32 v157, v242, v243\n" \
    "v_mul_f32 v236, v236, v240\n" \
    "v_mul_f32 v237, v237, v240\n" \
    "v_cvt_pk_bf16_f32 v158, v236, v237\n" \
    "v_mul_f32 v238, v238, v240\n" \
    "v_mul_f32 v239, v239, v240\n" \
    "v_cvt_pk_bf16_f32 v159, v238, v239\n" \
    "v_exp_f32 v68, v68\n" \
    "v_exp_f32 v69, v69\n" \
    "v_exp_f32 v70, v70\n" \
    "v_cvt_pk_bf16_f32 v58, v68, v69\n" \
    "v_exp_f32 v71, v71\n" \
    "v_exp_f32 v80, v80\n" \
    "v_cvt_pk_bf16_f32 v59, v70, v71\n" \
    "v_exp_f32 v81, v81\n" \
    "v_exp_f32 v82, v82\n" \
    "v_mfma_f32_16x16x32_bf16 v[240:243], v[196:199], v[56:59], 0\n" \
    "v_cvt_pk_bf16_f32 v208, v80, v81\n" \
    "v_exp_f32 v83, v83\n" \
    "v_mfma_f32_16x16x32_bf16 v[236:239], v[188:191], v[56:59], 0\n" \
    "v_cvt_pk_bf16_f32 v209, v82, v83\n" \
    "v_mov_b32 v220, 148752\n" \
    "v_mov_b32 v221, 1\n" \
    "v_mfma_f32_16x16x32_bf16 v[240:243], v[200:203], v[208:211], v[240:243]\n" \
    "s_mov_b64 exec, 1\n" \
    "ds_add_u32 v220, v221\n" \
    "s_mov_b64 exec, -1\n" \
    "v_mfma_f32_16x16x32_bf16 v[236:239], v[192:195], v[208:211], v[236:239]\n" \
    "v_exp_f32 v152, v152\n" \
    "s_cmp_lg_u32 s55, 0\n" \
    "s_cbranch_scc1 Lh2%=\n" \
    "Lr2%=:\n" \
    "ds_read_b128 v[172:175], v40\n" \
    "ds_read_b128 v[176:179], v40 offset:1024\n" \
    "ds_read_b128 v[180:183], v40 offset:2048\n" \
    "v_exp_f32 v153, v153\n" \
    "v_exp_f32 v154, v154\n" \
    "v_cvt_pk_bf16_f32 v152, v152, v153\n" \
    "v_rcp_f32 v240, v240\n" \
    "v_exp_f32 v155, v155\n" \
    "v_mul_f32 v242, v242, v240\n" \
    "v_cvt_pk_bf16_f32 v153, v154, v155\n" \
    "v_mul_f32 v243, v243, v240\n" \
    "v_exp_f32 v168, v168\n" \
    "v_cvt_pk_bf16_f32 v161, v242, v243\n" \
    "v_exp_f32 v169, v169\n" \
    "v_mul_f32 v236, v236, v240\n" \
    "v_cvt_pk_bf16_f32 v154, v168, v169\n" \
    "v_mul_f32 v237, v237, v240\n" \
    "v_exp_f32 v170, v170\n" \
    "v_cvt_pk_bf16_f32 v162, v236, v237\n" \
    "v_exp_f32 v171, v171\n" \
    "s_waitcnt lgkmcnt(2)\n" \
    "v_mfma_f32_16x16x32_bf16 v[60:63], v[172:175], v[224:227], 0\n" \
    "ds_read_b128 v[184:187], v40 offset:3072\n" \
    "v_mul_f32 v238, v238, v240\n" \
    "v_mfma_f32_16x16x32_bf16 v[64:67], v[172:175], v[228:231], 0\n" \
    "ds_read_b128 v[172:175], v40 offset:4096\n" \
    "v_cvt_pk_bf16_f32 v155, v170, v171\n" \
    "s_waitcnt lgkmcnt(2)\n" \
    "v_mfma_f32_16x16x32_bf16 v[60:63], v[176:179], v[140:143], v[60:63]\n" \
    "v_mul_f32 v239, v239, v240\n" \
    "v_exp_f32 v252, v252\n" \
    "v_mfma_f32_16x16x32_bf16 v[248:251], v[196:199], v[152:155], 0\n" \
    "v_cvt_pk_bf16_f32 v163, v238, v239\n" \
    "v_exp_f32 v253, v253\n" \
    "v_mfma_f32_16x16x32_bf16 v[244:247], v[188:191], v[152:155], 0\n" \
    "buffer_load_dwordx4 v[188:191], v39, s[44:47], s62 offen offset:1024\n" \
    "v_mfma_f32_16x16x32_bf16 v[64:67], v[176:179], v[144:147], v[64:67]\n" \
    "ds_read_b128 v[176:179], v40 offset:5120\n" \
    "v_cvt_pk_bf16_f32 v212, v252, v253\n" \
    "v_mfma_f32_16x16x32_bf16 v[60:63], v[180:183], v[156:159], v[60:63]\n" \
    "v_exp_f32 v254, v254\n" \
    "v_mfma_f32_16x16x32_bf16 v[64:67], v[180:183], v[160:163], v[64:67]\n" \
    "ds_read_b128 v[180:183], v40 offset:6144\n" \
    "v_min_u32 v252, s64, v39\n" \
    "s_waitcnt lgkmcnt(2)\n" \
    "v_mfma_f32_16x16x32_bf16 v[72:75], v[184:187], v[224:227], 0\n" \
    "v_exp_f32 v255, v255\n" \
    "v_mfma_f32_16x16x32_bf16 v[76:79], v[184:187], v[228:231], 0\n" \
    "ds_read_b128 v[184:187], v40 offset:7168\n" \
    "v_cvt_pk_bf16_f32 v213, v254, v255\n" \
    "v_mfma_f32_16x16x32_bf16 v[72:75], v[172:175], v[140:143], v[72:75]\n" \
    "v_add_u32 v44, 8256, v44\n" \
    "v_add_u32 v45, 8256, v45\n" \
    "v_mfma_f32_16x16x32_bf16 v[248:251], v[200:203], v[212:215], v[248:251]\n" \
    "v_max_u32 v200, s63, v39\n" \
    "v_add_u32 v46, 8256, v46\n" \
    "v_mfma_f32_16x16x32_bf16 v[244:247], v[192:195], v[212:215], v[244:247]\n" \
    "buffer_load_dwordx4 v[192:195], v39, s[44:47], s62 offen offset:2048\n" \
    "v_mfma_f32_16x16x32_bf16 v[76:79], v[172:175], v[144:147], v[76:79]\n" \
    "buffer_load_dwordx4 v[196:199], v39, s[44:47], s62 offen offset:3072\n" \
    "s_waitcnt lgkmcnt(2)\n" \
    "v_mfma_f32_16x16x32_bf16 v[72:75], v[176:179], v[156:159], v[72:75]\n" \
    "buffer_load_dwordx4 v[200:203], v200, s[44:47], s62 offen\n" \
    "v_mfma_f32_16x16x32_bf16 v[76:79], v[176:179], v[160:163], v[76:79]\n" \
    "buffer_load_dwordx4 v[252:255], v252, s[44:47], s65 offen\n" \
    "s_waitcnt lgkmcnt(1)\n" \
    "v_mfma_f32_16x16x32_bf16 v[84:87], v[180:183], v[224:227], 0\n" \
    "ds_read_b128 v[172:175], v40 offset:8192\n" \
    "ds_read_b128 v[176:179], v40 offset:9216\n" \
    "v_mfma_f32_16x16x32_bf16 v[88:91], v[180:183], v[228:231], 0\n" \
    "ds_read_b128 v[180:183], v40 offset:10240\n" \
    "v_rcp_f32 v248, v248\n" \
    "s_waitcnt lgkmcnt(3)\n" \
    "v_mfma_f32_16x16x32_bf16 v[84:87], v[184:187], v[140:143], v[84:87]\n" \
    "v_mul_f32 v250, v250, v248\n" \
    "v_mul_f32 v251, v251, v248\n" \
    "v_mfma_f32_16x16x32_bf16 v[88:91], v[184:187], v[144:147], v[88:91]\n" \
    "ds_read_b128 v[184:187], v40 offset:11264\n" \
    "v_add_u32 v47, 8256, v47\n" \
    "v_cvt_pk_bf16_f32 v165, v250, v251\n" \
    "v_add_u32 v54, 8256, v54\n" \
    "v_mul_f32 v244, v244, v248\n" \
    "v_mul_f32 v245, v245, v248\n" \
    "v_cvt_pk_bf16_f32 v166, v244, v245\n" \
    "v_mul_f32 v246, v246, v248\n" \
    "v_mul_f32 v247, v247, v248\n" \
    "v_cvt_pk_bf16_f32 v167, v246, v247\n" \
    "v_fmaak_f32 v152, v60, v60, 0x400d8ec9\n" \
    "v_fmac_f32 v152, v61, v61\n" \
    "v_fmac_f32 v152, v62, v62\n" \
    "v_fmac_f32 v152, v63, v63\n" \
    "s_waitcnt lgkmcnt(3)\n" \
    "v_mfma_f32_16x16x32_bf16 v[84:87], v[172:175], v[156:159], v[84:87]\n" \
    "v_fmac_f32 v152, v72, v72\n" \
    "v_fmac_f32 v152, v73, v73\n" \
    "v_mfma_f32_16x16x32_bf16 v[88:91], v[172:175], v[160:163], v[88:91]\n" \
    "ds_read_b128 v[172:175], v40 offset:12288\n" \
    "v_fmac_f32 v152, v74, v74\n" \
    "s_waitcnt lgkmcnt(2)\n" \
    "v_mfma_f32_16x16x32_bf16 v[56:59], v[176:179], v[224:227], 0\n" \
    "v_fmac_f32 v152, v75, v75\n" \
    "v_fmac_f32 v152, v84, v84\n" \
    "v_mfma_f32_16x16x32_bf16 v[68:71], v[176:179], v[228:231], 0\n" \
    "ds_read_b128 v[176:179], v40 offset:13312\n" \
    "v_fmac_f32 v152, v85, v85\n" \
    "v_mfma_f32_16x16x32_bf16 v[56:59], v[180:183], v[140:143], v[56:59]\n" \
    "v_fmac_f32 v152, v86, v86\n" \
    "v_fmac_f32 v152, v87, v87\n" \
    "v_mfma_f32_16x16x32_bf16 v[68:71], v[180:183], v[144:147], v[68:71]\n" \
    "ds_read_b128 v[180:183], v40 offset:14336\n" \
    "v_fmaak_f32 v153, v64, v64, 0x400d8ec9\n" \
    "s_waitcnt lgkmcnt(3)\n" \
    "v_mfma_f32_16x16x32_bf16 v[56:59], v[184:187], v[156:159], v[56:59]\n" \
    "v_fmac_f32 v153, v65, v65\n" \
    "v_fmac_f32 v153, v66, v66\n" \
    "v_mfma_f32_16x16x32_bf16 v[68:71], v[184:187], v[160:163], v[68:71]\n" \
    "ds_read_b128 v[184:187], v40 offset:15360\n" \
    "v_fmac_f32 v153, v67, v67\n" \
    "s_waitcnt lgkmcnt(3)\n" \
    "v_mfma_f32_16x16x32_bf16 v[80:83], v[172:175], v[224:227], 0\n" \
    "v_fmac_f32 v153, v76, v76\n" \
    "v_fmac_f32 v152, v56, v56\n" \
    "v_mfma_f32_16x16x32_bf16 v[204:207], v[172:175], v[228:231], 0\n" \
    "ds_read_b128 v[172:175], v40 offset:16384\n" \
    "v_fmac_f32 v152, v57, v57\n" \
    "s_waitcnt lgkmcnt(3)\n" \
    "v_mfma_f32_16x16x32_bf16 v[80:83], v[176:179], v[140:143], v[80:83]\n" \
    "v_fmac_f32 v152, v58, v58\n" \
    "v_fmac_f32 v152, v59, v59\n" \
    "v_mfma_f32_16x16x32_bf16 v[204:207], v[176:179], v[144:147], v[204:207]\n" \
    "ds_read_b128 v[176:179], v40 offset:17408\n" \
    "v_fmac_f32 v153, v77, v77\n" \
    "s_waitcnt lgkmcnt(3)\n" \
    "v_mfma_f32_16x16x32_bf16 v[80:83], v[180:183], v[156:159], v[80:83]\n" \
    "v_fmac_f32 v153, v78, v78\n" \
    "v_fmac_f32 v153, v79, v79\n" \
    "v_mfma_f32_16x16x32_bf16 v[204:207], v[180:183], v[160:163], v[204:207]\n" \
    "ds_read_b128 v[180:183], v40\n" \
    "v_fmac_f32 v153, v88, v88\n" \
    "s_waitcnt lgkmcnt(3)\n" \
    "v_mfma_f32_16x16x32_bf16 v[208:211], v[184:187], v[224:227], 0\n" \
    "v_fmac_f32 v153, v89, v89\n" \
    "v_fmac_f32 v152, v80, v80\n" \
    "v_mfma_f32_16x16x32_bf16 v[220:223], v[184:187], v[228:231], 0\n" \
    "ds_read_b128 v[184:187], v40 offset:1024\n" \
    "v_fmac_f32 v152, v81, v81\n" \
    "s_waitcnt lgkmcnt(3)\n" \
    "v_mfma_f32_16x16x32_bf16 v[208:211], v[172:175], v[140:143], v[208:211]\n" \
    "v_fmac_f32 v152, v82, v82\n" \
    "v_fmac_f32 v152, v83, v83\n" \
    "v_mfma_f32_16x16x32_bf16 v[220:223], v[172:175], v[144:147], v[220:223]\n" \
    "ds_read_b128 v[172:175], v40 offset:2048\n" \
    "v_fmac_f32 v153, v90, v90\n" \
    "s_waitcnt lgkmcnt(3)\n" \
    "v_mfma_f32_16x16x32_bf16 v[208:211], v[176:179], v[156:159], v[208:211]\n" \
    "v_fmac_f32 v153, v91, v91\n" \
    "v_fmac_f32 v153, v68, v68\n" \
    "v_mfma_f32_16x16x32_bf16 v[220:223], v[176:179], v[160:163], v[220:223]\n" \
    "ds_read_b128 v[176:179], v40 offset:3072\n" \
    "v_fmac_f32 v153, v69, v69\n" \
    "s_waitcnt lgkmcnt(3)\n" \
    "v_mfma_f32_16x16x32_bf16 v[216:219], v[180:183], v[232:235], 0\n" \
    "ds_read_b128 v[180:183], v40 offset:4096\n" \
    "v_fmac_f32 v152, v208, v208\n" \
    "v_fmac_f32 v152, v209, v209\n" \
    "v_fmac_f32 v152, v210, v210\n" \
    "v_fmac_f32 v152, v211, v211\n" \
    "s_waitcnt lgkmcnt(3)\n" \
    "v_mfma_f32_16x16x32_bf16 v[216:219], v[184:187], v[148:151], v[216:219]\n" \
    "ds_read_b128 v[184:187], v40 offset:5120\n" \
    "v_mov_b32 v155, v152\n" \
    "v_fmac_f32 v153, v70, v70\n" \
    "v_fmac_f32 v153, v71, v71\n" \
    "v_permlane32_swap_b32 v152, v155\n" \
    "s_waitcnt lgkmcnt(3)\n" \
    "v_mfma_f32_16x16x32_bf16 v[216:219], v[172:175], v[164:167], v[216:219]\n" \
    "ds_read_b128 v[172:175], v40 offset:6144\n" \
    "v_add_f32 v152, v152, v155\n" \
    "v_mov_b32 v155, v152\n" \
    "v_fmac_f32 v153, v204, v204\n" \
    "v_fmac_f32 v153, v205, v205\n" \
    "v_permlane16_swap_b32 v152, v155\n" \
    "s_waitcnt lgkmcnt(3)\n" \
    "v_mfma_f32_16x16x32_bf16 v[212:215], v[176:179], v[232:235], 0\n" \
    "ds_read_b128 v[176:179], v40 offset:7168\n" \
    "v_add_f32 v152, v152, v155\n" \
    "s_waitcnt lgkmcnt(3)\n" \
    "v_mfma_f32_16x16x32_bf16 v[212:215], v[180:183], v[148:151], v[212:215]\n" \
    "ds_read_b128 v[180:183], v40 offset:8192\n" \
    "v_mul_f32 v152, 0x3c2aaaab, v152\n" \
    "v_rsq_f32 v168, v152\n" \
    "s_waitcnt lgkmcnt(3)\n" \
    "v_mfma_f32_16x16x32_bf16 v[212:215], v[184:187], v[164:167], v[212:215]\n" \
    "ds_read_b128 v[184:187], v40 offset:9216\n" \
    "v_mul_f32 v60, v60, v168\n" \
    "v_mul_f32 v61, v61, v168\n" \
    "v_cvt_pk_bf16_f32 v60, v60, v61\n" \
    "v_mul_f32 v62, v62, v168\n" \
    "v_mul_f32 v63, v63, v168\n" \
    "s_waitcnt lgkmcnt(3)\n" \
    "v_mfma_f32_16x16x32_bf16 v[236:239], v[172:175], v[232:235], 0\n" \
    "ds_read_b128 v[172:175], v40 offset:10240\n" \
    "v_cvt_pk_bf16_f32 v61, v62, v63\n" \
    "v_mul_f32 v72, v72, v168\n" \
    "v_mul_f32 v73, v73, v168\n" \
    "v_cvt_pk_bf16_f32 v62, v72, v73\n" \
    "v_mul_f32 v74, v74, v168\n" \
    "v_mul_f32 v75, v75, v168\n" \
    "s_waitcnt lgkmcnt(3)\n" \
    "v_mfma_f32_16x16x32_bf16 v[236:239], v[176:179], v[148:151], v[236:239]\n" \
    "ds_read_b128 v[176:179], v40 offset:11264\n" \
    "v_cvt_pk_bf16_f32 v63, v74, v75\n" \
    "s_waitcnt lgkmcnt(3)\n" \
    "v_mfma_f32_16x16x32_bf16 v[236:239], v[180:183], v[164:167], v[236:239]\n" \
    "buffer_store_dwordx4 v[60:63], v36, s[48:51], s56 offen sc0 sc1\n" \
    "s_waitcnt lgkmcnt(2)\n" \
    "v_mfma_f32_16x16x32_bf16 v[240:243], v[184:187], v[232:235], 0\n" \
    "ds_read_b128 v[180:183], v40 offset:12288\n" \
    "ds_read_b128 v[184:187], v40 offset:13312\n" \
    "v_mul_f32 v84, v84, v168\n" \
    "v_mul_f32 v85, v85, v168\n" \
    "v_cvt_pk_bf16_f32 v84, v84, v85\n" \
    "s_waitcnt lgkmcnt(3)\n" \
    "v_mfma_f32_16x16x32_bf16 v[240:243], v[172:175], v[148:151], v[240:243]\n" \
    "ds_read_b128 v[172:175], v40 offset:14336\n" \
    "v_mul_f32 v86, v86, v168\n" \
    "v_mul_f32 v87, v87, v168\n" \
    "v_cvt_pk_bf16_f32 v85, v86, v87\n" \
    "v_mul_f32 v56, v56, v168\n" \
    "v_mul_f32 v57, v57, v168\n" \
    "v_cvt_pk_bf16_f32 v86, v56, v57\n" \
    "s_waitcnt lgkmcnt(3)\n" \
    "v_mfma_f32_16x16x32_bf16 v[240:243], v[176:179], v[164:167], v[240:243]\n" \
    "ds_read_b128 v[176:179], v40 offset:15360\n" \
    "v_mul_f32 v58, v58, v168\n" \
    "v_mul_f32 v59, v59, v168\n" \
    "v_cvt_pk_bf16_f32 v87, v58, v59\n" \
    "v_mul_f32 v80, v80, v168\n" \
    "v_mul_f32 v81, v81, v168\n" \
    "v_cvt_pk_bf16_f32 v80, v80, v81\n" \
    "v_mul_f32 v82, v82, v168\n" \
    "v_mul_f32 v83, v83, v168\n" \
    "v_cvt_pk_bf16_f32 v81, v82, v83\n" \
    "s_waitcnt lgkmcnt(3)\n" \
    "v_mfma_f32_16x16x32_bf16 v[244:247], v[180:183], v[232:235], 0\n" \
    "buffer_store_dwordx4 v[84:87], v36, s[48:51], s56 offen offset:64 sc0 sc1\n" \
    "s_waitcnt lgkmcnt(1)\n" \
    "v_mfma_f32_16x16x32_bf16 v[244:247], v[184:187], v[148:151], v[244:247]\n" \
    "ds_read_b128 v[180:183], v40 offset:16384\n" \
    "ds_read_b128 v[184:187], v40 offset:17408\n" \
    "v_mfma_f32_16x16x32_bf16 v[244:247], v[172:175], v[164:167], v[244:247]\n" \
    "v_mul_f32 v208, v208, v168\n" \
    "v_mul_f32 v209, v209, v168\n" \
    "v_cvt_pk_bf16_f32 v82, v208, v209\n" \
    "s_waitcnt lgkmcnt(2)\n" \
    "v_mfma_f32_16x16x32_bf16 v[248:251], v[176:179], v[232:235], 0\n" \
    "v_mul_f32 v210, v210, v168\n" \
    "v_mul_f32 v211, v211, v168\n" \
    "v_cvt_pk_bf16_f32 v83, v210, v211\n" \
    "v_fmac_f32 v153, v206, v206\n" \
    "v_fmac_f32 v153, v207, v207\n" \
    "v_fmac_f32 v153, v220, v220\n" \
    "v_fmac_f32 v153, v221, v221\n" \
    "v_fmac_f32 v153, v222, v222\n" \
    "v_fmac_f32 v153, v223, v223\n" \
    "v_mov_b32 v155, v153\n" \
    "buffer_store_dwordx4 v[80:83], v36, s[48:51], s56 offen offset:128 sc0 sc1\n" \
    "v_fmaak_f32 v154, v216, v216, 0x400d8ec9\n" \
    "s_waitcnt lgkmcnt(1)\n" \
    "v_mfma_f32_16x16x32_bf16 v[248:251], v[180:183], v[148:151], v[248:251]\n" \
    "v_permlane32_swap_b32 v153, v155\n" \
    "s_waitcnt lgkmcnt(0)\n" \
    "v_mfma_f32_16x16x32_bf16 v[248:251], v[184:187], v[164:167], v[248:251]\n" \
    "v_add_f32 v153, v153, v155\n" \
    "v_mov_b32 v155, v153\n" \
    "v_fmac_f32 v154, v217, v217\n" \
    "v_fmac_f32 v154, v218, v218\n" \
    "v_permlane16_swap_b32 v153, v155\n" \
    "v_add_f32 v153, v153, v155\n" \
    "v_mul_f32 v153, 0x3c2aaaab, v153\n" \
    "v_rsq_f32 v169, v153\n" \
    "v_fmac_f32 v154, v219, v219\n" \
    "v_mul_f32 v64, v64, v169\n" \
    "v_mul_f32 v65, v65, v169\n" \
    "v_cvt_pk_bf16_f32 v64, v64, v65\n" \
    "v_mul_f32 v66, v66, v169\n" \
    "v_mul_f32 v67, v67, v169\n" \
    "v_cvt_pk_bf16_f32 v65, v66, v67\n" \
    "v_mul_f32 v76, v76, v169\n" \
    "v_mul_f32 v77, v77, v169\n" \
    "v_cvt_pk_bf16_f32 v66, v76, v77\n" \
    "v_mul_f32 v78, v78, v169\n" \
    "v_mul_f32 v79, v79, v169\n" \
    "v_cvt_pk_bf16_f32 v67, v78, v79\n" \
    "v_mul_f32 v88, v88, v169\n" \
    "v_mul_f32 v89, v89, v169\n" \
    "v_cvt_pk_bf16_f32 v88, v88, v89\n" \
    "v_mul_f32 v90, v90, v169\n" \
    "v_mul_f32 v91, v91, v169\n" \
    "v_cvt_pk_bf16_f32 v89, v90, v91\n" \
    "v_mul_f32 v68, v68, v169\n" \
    "v_mul_f32 v69, v69, v169\n" \
    "v_cvt_pk_bf16_f32 v90, v68, v69\n" \
    "v_mul_f32 v70, v70, v169\n" \
    "v_mul_f32 v71, v71, v169\n" \
    "v_cvt_pk_bf16_f32 v91, v70, v71\n" \
    "v_mul_f32 v204, v204, v169\n" \
    "v_mul_f32 v205, v205, v169\n" \
    "v_cvt_pk_bf16_f32 v204, v204, v205\n" \
    "v_mul_f32 v206, v206, v169\n" \
    "v_mul_f32 v207, v207, v169\n" \
    "v_cvt_pk_bf16_f32 v205, v206, v207\n" \
    "v_mul_f32 v220, v220, v169\n" \
    "v_mul_f32 v221, v221, v169\n" \
    "v_cvt_pk_bf16_f32 v206, v220, v221\n" \
    "v_mul_f32 v222, v222, v169\n" \
    "v_mul_f32 v223, v223, v169\n" \
    "v_cvt_pk_bf16_f32 v207, v222, v223\n" \
    "buffer_store_dwordx4 v[64:67], v37, s[48:51], s56 offen sc0 sc1\n" \
    "buffer_store_dwordx4 v[88:91], v37, s[48:51], s56 offen offset:64 sc0 sc1\n" \
    "buffer_store_dwordx4 v[204:207], v37, s[48:51], s56 offen offset:128 sc0 sc1\n" \
    "v_fmac_f32 v154, v212, v212\n" \
    "v_fmac_f32 v154, v213, v213\n" \
    "v_fmac_f32 v154, v214, v214\n" \
    "v_fmac_f32 v154, v215, v215\n" \
    "v_fmac_f32 v154, v236, v236\n" \
    "v_fmac_f32 v154, v237, v237\n" \
    "v_fmac_f32 v154, v238, v238\n" \
    "v_fmac_f32 v154, v239, v239\n" \
    "v_fmac_f32 v154, v240, v240\n" \
    "v_fmac_f32 v154, v241, v241\n" \
    "v_fmac_f32 v154, v242, v242\n" \
    "v_fmac_f32 v154, v243, v243\n" \
    "v_fmac_f32 v154, v244, v244\n" \
    "v_fmac_f32 v154, v245, v245\n" \
    "v_fmac_f32 v154, v246, v246\n" \
    "v_fmac_f32 v154, v247, v247\n" \
    "v_fmac_f32 v154, v248, v248\n" \
    "v_fmac_f32 v154, v249, v249\n" \
    "v_fmac_f32 v154, v250, v250\n" \
    "v_fmac_f32 v154, v251, v251\n" \
    "v_mov_b32 v155, v154\n" \
    "s_nop 1\n" \
    "v_permlane32_swap_b32 v154, v155\n" \
    "v_add_f32 v154, v154, v155\n" \
    "v_mov_b32 v155, v154\n" \
    "s_nop 1\n" \
    "v_permlane16_swap_b32 v154, v155\n" \
    "v_add_f32 v154, v154, v155\n" \
    "v_mul_f32 v154, 0x3c2aaaab, v154\n" \
    "v_rsq_f32 v170, v154\n" \
    "s_nop 0\n" \
    "v_mul_f32 v216, v216, v170\n" \
    "v_mul_f32 v217, v217, v170\n" \
    "v_cvt_pk_bf16_f32 v216, v216, v217\n" \
    "v_mul_f32 v218, v218, v170\n" \
    "v_mul_f32 v219, v219, v170\n" \
    "v_cvt_pk_bf16_f32 v217, v218, v219\n" \
    "v_mul_f32 v212, v212, v170\n" \
    "v_mul_f32 v213, v213, v170\n" \
    "v_cvt_pk_bf16_f32 v218, v212, v213\n" \
    "v_mul_f32 v214, v214, v170\n" \
    "v_mul_f32 v215, v215, v170\n" \
    "v_cvt_pk_bf16_f32 v219, v214, v215\n" \
    "v_mul_f32 v236, v236, v170\n" \
    "v_mul_f32 v237, v237, v170\n" \
    "v_cvt_pk_bf16_f32 v236, v236, v237\n" \
    "v_mul_f32 v238, v238, v170\n" \
    "v_mul_f32 v239, v239, v170\n" \
    "v_cvt_pk_bf16_f32 v237, v238, v239\n" \
    "v_mul_f32 v240, v240, v170\n" \
    "v_mul_f32 v241, v241, v170\n" \
    "v_cvt_pk_bf16_f32 v238, v240, v241\n" \
    "v_mul_f32 v242, v242, v170\n" \
    "v_mul_f32 v243, v243, v170\n" \
    "v_cvt_pk_bf16_f32 v239, v242, v243\n" \
    "v_mul_f32 v244, v244, v170\n" \
    "v_mul_f32 v245, v245, v170\n" \
    "v_cvt_pk_bf16_f32 v244, v244, v245\n" \
    "v_mul_f32 v246, v246, v170\n" \
    "v_mul_f32 v247, v247, v170\n" \
    "v_cvt_pk_bf16_f32 v245, v246, v247\n" \
    "v_mul_f32 v248, v248, v170\n" \
    "v_mul_f32 v249, v249, v170\n" \
    "v_cvt_pk_bf16_f32 v246, v248, v249\n" \
    "v_mul_f32 v250, v250, v170\n" \
    "v_mul_f32 v251, v251, v170\n" \
    "v_cvt_pk_bf16_f32 v247, v250, v251\n" \
    "buffer_store_dwordx4 v[216:219], v38, s[48:51], s56 offen sc0 sc1\n" \
    "buffer_store_dwordx4 v[236:239], v38, s[48:51], s56 offen offset:64 sc0 sc1\n" \
    "buffer_store_dwordx4 v[244:247], v38, s[48:51], s56 offen offset:128 sc0 sc1\n" \
    "s_mov_b32 s52, s66\n" \
    "s_mov_b32 s56, s57\n" \
    "s_mov_b32 s55, 0\n" \
    "s_add_u32 s35, s35, 4\n" \
    "s_sub_u32 s54, s54, 1\n" \
    "s_cmp_lg_u32 s54, 0\n" \
    "s_cbranch_scc1 Ltop%=\n" \
    "Lfin%=:\n" \
    "v_mov_b32 v55, 148752\n" \
    "v_mov_b32 v54, 1\n" \
    "ds_read_b32 v52, v55\n" \
    "s_waitcnt lgkmcnt(0)\n" \
    "v_cmp_ne_u32 vcc, s34, v52\n" \
    "s_nop 1\n" \
    "s_cbranch_vccnz Ldone%=\n" \
    "s_mov_b64 exec, 1\n" \
    "ds_add_rtn_u32 v52, v55, v54 offset:4\n" \
    "s_waitcnt lgkmcnt(0)\n" \
    "v_cmp_ne_u32 vcc, 0, v52\n" \
    "s_mov_b64 exec, -1\n" \
    "s_nop 1\n" \
    "s_cbranch_vccnz Ldone%=\n" \
    "s_setprio 3\n" \
    "v_mov_b32 v56, 148744\n" \
    "s_branch Lw3e%=\n" \
    "Lw3%=:\n" \
    "s_sleep 1\n" \
    "Lw3e%=:\n" \
    "ds_read_b32 v57, v56\n" \
    "s_waitcnt lgkmcnt(0)\n" \
    "v_cmp_gt_u32 vcc, 1, v57\n" \
    "s_nop 1\n" \
    "s_cbranch_vccnz Lw3%=\n" \
    "ds_read_b128 v[72:75], v40\n" \
    "ds_read_b128 v[76:79], v40 offset:3072\n" \
    "ds_read_b128 v[80:83], v40 offset:6144\n" \
    "ds_read_b128 v[84:87], v40 offset:9216\n" \
    "ds_read_b128 v[88:91], v40 offset:12288\n" \
    "ds_read_b128 v[172:175], v40 offset:15360\n" \
    "ds_read_b128 v[176:179], v40 offset:1024\n" \
    "ds_read_b128 v[180:183], v40 offset:4096\n" \
    "v_mul_u32_u24 v56, 516, v43\n" \
    "v_bfe_u32 v58, v39, 8, 2\n" \
    "v_lshlrev_b32 v58, 4, v58\n" \
    "v_add_u32 v56, v56, v58\n" \
    "v_add_u32 v56, 116224, v56\n" \
    "v_add_u32 v57, 148800, v43\n" \
    "ds_read_b32 v57, v57\n" \
    "ds_read_b128 v[140:143], v56\n" \
    "ds_read_b128 v[144:147], v56 offset:256\n" \
    "ds_read_b128 v[184:187], v40 offset:7168\n" \
    "ds_read_b128 v[228:231], v40 offset:10240\n" \
    "ds_read_b128 v[232:235], v40 offset:13312\n" \
    "ds_read_b128 v[236:239], v40 offset:16384\n" \
    "s_waitcnt lgkmcnt(6)\n" \
    "v_add_u32 v57, v57, v58\n" \
    "ds_read_b128 v[148:151], v56 offset:64\n" \
    "ds_read_b128 v[152:155], v56 offset:320\n" \
    "ds_read_b128 v[156:159], v56 offset:128\n" \
    "ds_read_b128 v[160:163], v56 offset:384\n" \
    "ds_read_b128 v[164:167], v56 offset:192\n" \
    "ds_read_b128 v[168:171], v56 offset:448\n" \
    "s_waitcnt lgkmcnt(6)\n" \
    "v_rcp_f32 v146, v146\n" \
    "s_nop 0\n" \
    "v_mul_f32 v140, v140, v146\n" \
    "v_mul_f32 v141, v141, v146\n" \
    "v_cvt_pk_bf16_f32 v60, v140, v141\n" \
    "v_mul_f32 v142, v142, v146\n" \
    "v_mul_f32 v143, v143, v146\n" \
    "v_cvt_pk_bf16_f32 v61, v142, v143\n" \
    "v_mul_f32 v144, v144, v146\n" \
    "v_mul_f32 v145, v145, v146\n" \
    "v_cvt_pk_bf16_f32 v62, v144, v145\n" \
    "s_waitcnt lgkmcnt(4)\n" \
    "v_rcp_f32 v152, v152\n" \
    "s_waitcnt lgkmcnt(2)\n" \
    "v_rcp_f32 v162, v162\n" \
    "v_mul_f32 v154, v154, v152\n" \
    "v_mul_f32 v155, v155, v152\n" \
    "v_cvt_pk_bf16_f32 v63, v154, v155\n" \
    "v_mul_f32 v148, v148, v152\n" \
    "v_mul_f32 v149, v149, v152\n" \
    "v_mfma_f32_16x16x32_bf16 v[204:207], v[72:75], v[60:63], 0\n" \
    "ds_read_b128 v[72:75], v40 offset:2048\n" \
    "v_cvt_pk_bf16_f32 v64, v148, v149\n" \
    "v_mfma_f32_16x16x32_bf16 v[208:211], v[76:79], v[60:63], 0\n" \
    "ds_read_b128 v[76:79], v40 offset:5120\n" \
    "v_mul_f32 v150, v150, v152\n" \
    "v_mfma_f32_16x16x32_bf16 v[212:215], v[80:83], v[60:63], 0\n" \
    "ds_read_b128 v[80:83], v40 offset:8192\n" \
    "v_mul_f32 v151, v151, v152\n" \
    "v_mfma_f32_16x16x32_bf16 v[216:219], v[84:87], v[60:63], 0\n" \
    "ds_read_b128 v[84:87], v40 offset:11264\n" \
    "v_cvt_pk_bf16_f32 v65, v150, v151\n" \
    "v_mfma_f32_16x16x32_bf16 v[220:223], v[88:91], v[60:63], 0\n" \
    "ds_read_b128 v[88:91], v40 offset:14336\n" \
    "v_mul_f32 v156, v156, v162\n" \
    "v_mfma_f32_16x16x32_bf16 v[224:227], v[172:175], v[60:63], 0\n" \
    "ds_read_b128 v[172:175], v40 offset:17408\n" \
    "v_mul_f32 v157, v157, v162\n" \
    "v_cvt_pk_bf16_f32 v66, v156, v157\n" \
    "v_mul_f32 v158, v158, v162\n" \
    "v_mul_f32 v159, v159, v162\n" \
    "v_cvt_pk_bf16_f32 v67, v158, v159\n" \
    "v_mul_f32 v160, v160, v162\n" \
    "v_mul_f32 v161, v161, v162\n" \
    "v_mfma_f32_16x16x32_bf16 v[204:207], v[176:179], v[64:67], v[204:207]\n" \
    "v_cvt_pk_bf16_f32 v68, v160, v161\n" \
    "s_waitcnt lgkmcnt(5)\n" \
    "v_rcp_f32 v168, v168\n" \
    "v_mfma_f32_16x16x32_bf16 v[208:211], v[180:183], v[64:67], v[208:211]\n" \
    "v_mul_f32 v170, v170, v168\n" \
    "v_mul_f32 v171, v171, v168\n" \
    "v_mfma_f32_16x16x32_bf16 v[212:215], v[184:187], v[64:67], v[212:215]\n" \
    "v_cvt_pk_bf16_f32 v69, v170, v171\n" \
    "v_mul_f32 v164, v164, v168\n" \
    "v_mfma_f32_16x16x32_bf16 v[216:219], v[228:231], v[64:67], v[216:219]\n" \
    "v_mul_f32 v165, v165, v168\n" \
    "v_cvt_pk_bf16_f32 v70, v164, v165\n" \
    "v_mfma_f32_16x16x32_bf16 v[220:223], v[232:235], v[64:67], v[220:223]\n" \
    "v_mul_f32 v166, v166, v168\n" \
    "v_mul_f32 v167, v167, v168\n" \
    "v_mfma_f32_16x16x32_bf16 v[224:227], v[236:239], v[64:67], v[224:227]\n" \
    "v_cvt_pk_bf16_f32 v71, v166, v167\n" \
    "s_nop 1\n" \
    "v_mfma_f32_16x16x32_bf16 v[204:207], v[72:75], v[68:71], v[204:207]\n" \
    "s_waitcnt lgkmcnt(0)\n" \
    "v_mfma_f32_16x16x32_bf16 v[208:211], v[76:79], v[68:71], v[208:211]\n" \
    "v_mfma_f32_16x16x32_bf16 v[212:215], v[80:83], v[68:71], v[212:215]\n" \
    "v_mfma_f32_16x16x32_bf16 v[216:219], v[84:87], v[68:71], v[216:219]\n" \
    "v_mfma_f32_16x16x32_bf16 v[220:223], v[88:91], v[68:71], v[220:223]\n" \
    "v_mfma_f32_16x16x32_bf16 v[224:227], v[172:175], v[68:71], v[224:227]\n" \
    "s_nop 1\n" \
    "v_fmaak_f32 v240, v204, v204, 0x400d8ec9\n" \
    "v_fmac_f32 v240, v205, v205\n" \
    "v_fmac_f32 v240, v206, v206\n" \
    "v_fmac_f32 v240, v207, v207\n" \
    "v_fmac_f32 v240, v208, v208\n" \
    "v_fmac_f32 v240, v209, v209\n" \
    "v_fmac_f32 v240, v210, v210\n" \
    "v_fmac_f32 v240, v211, v211\n" \
    "v_fmac_f32 v240, v212, v212\n" \
    "v_fmac_f32 v240, v213, v213\n" \
    "v_fmac_f32 v240, v214, v214\n" \
    "v_fmac_f32 v240, v215, v215\n" \
    "v_fmac_f32 v240, v216, v216\n" \
    "v_fmac_f32 v240, v217, v217\n" \
    "v_fmac_f32 v240, v218, v218\n" \
    "v_fmac_f32 v240, v219, v219\n" \
    "v_fmac_f32 v240, v220, v220\n" \
    "v_fmac_f32 v240, v221, v221\n" \
    "v_fmac_f32 v240, v222, v222\n" \
    "v_fmac_f32 v240, v223, v223\n" \
    "v_fmac_f32 v240, v224, v224\n" \
    "v_fmac_f32 v240, v225, v225\n" \
    "v_fmac_f32 v240, v226, v226\n" \
    "v_fmac_f32 v240, v227, v227\n" \
    "v_mov_b32 v241, v240\n" \
    "s_nop 1\n" \
    "v_permlane32_swap_b32 v240, v241\n" \
    "v_add_f32 v240, v240, v241\n" \
    "v_mov_b32 v241, v240\n" \
    "s_nop 1\n" \
    "v_permlane16_swap_b32 v240, v241\n" \
    "v_add_f32 v240, v240, v241\n" \
    "v_mul_f32 v240, 0x3c2aaaab, v240\n" \
    "v_rsq_f32 v242, v240\n" \
    "s_nop 0\n" \
    "v_mul_f32 v204, v204, v242\n" \
    "v_mul_f32 v205, v205, v242\n" \
    "v_cvt_pk_bf16_f32 v204, v204, v205\n" \
    "v_mul_f32 v206, v206, v242\n" \
    "v_mul_f32 v207, v207, v242\n" \
    "v_cvt_pk_bf16_f32 v205, v206, v207\n" \
    "v_mul_f32 v208, v208, v242\n" \
    "v_mul_f32 v209, v209, v242\n" \
    "v_cvt_pk_bf16_f32 v206, v208, v209\n" \
    "v_mul_f32 v210, v210, v242\n" \
    "v_mul_f32 v211, v211, v242\n" \
    "v_cvt_pk_bf16_f32 v207, v210, v211\n" \
    "v_mul_f32 v212, v212, v242\n" \
    "v_mul_f32 v213, v213, v242\n" \
    "v_cvt_pk_bf16_f32 v212, v212, v213\n" \
    "v_mul_f32 v214, v214, v242\n" \
    "v_mul_f32 v215, v215, v242\n" \
    "v_cvt_pk_bf16_f32 v213, v214, v215\n" \
    "v_mul_f32 v216, v216, v242\n" \
    "v_mul_f32 v217, v217, v242\n" \
    "v_cvt_pk_bf16_f32 v214, v216, v217\n" \
    "v_mul_f32 v218, v218, v242\n" \
    "v_mul_f32 v219, v219, v242\n" \
    "v_cvt_pk_bf16_f32 v215, v218, v219\n" \
    "v_mul_f32 v220, v220, v242\n" \
    "v_mul_f32 v221, v221, v242\n" \
    "v_cvt_pk_bf16_f32 v220, v220, v221\n" \
    "v_mul_f32 v222, v222, v242\n" \
    "v_mul_f32 v223, v223, v242\n" \
    "v_cvt_pk_bf16_f32 v221, v222, v223\n" \
    "v_mul_f32 v224, v224, v242\n" \
    "v_mul_f32 v225, v225, v242\n" \
    "v_cvt_pk_bf16_f32 v222, v224, v225\n" \
    "v_mul_f32 v226, v226, v242\n" \
    "v_mul_f32 v227, v227, v242\n" \
    "v_cvt_pk_bf16_f32 v223, v226, v227\n" \
    "buffer_store_dwordx4 v[204:207], v57, s[48:51], 0 offen sc0 sc1\n" \
    "buffer_store_dwordx4 v[212:215], v57, s[48:51], 0 offen offset:64 sc0 sc1\n" \
    "buffer_store_dwordx4 v[220:223], v57, s[48:51], 0 offen offset:128 sc0 sc1\n" \
    "Ldone%=:\n" \
    "s_cmp_lg_u32 s54, 0\n" \
    "s_cbranch_scc1 Lout%=\n" \
    "s_endpgm\n" \
    "Lh1%=:\n" \
    "v_mov_b32 v252, 148740\n" \
    "s_branch Lw1e%=\n" \
    "Lw1%=:\n" \
    "s_sleep 1\n" \
    "Lw1e%=:\n" \
    "ds_read_b32 v253, v252\n" \
    "s_waitcnt lgkmcnt(0)\n" \
    "v_cmp_gt_u32 vcc, 4, v253\n" \
    "s_nop 1\n" \
    "s_cbranch_vccnz Lw1%=\n" \
    "s_branch Lr1%=\n" \
    "Lh2%=:\n" \
    "v_mov_b32 v172, 148744\n" \
    "s_branch Lw2e%=\n" \
    "Lw2%=:\n" \
    "s_sleep 1\n" \
    "Lw2e%=:\n" \
    "ds_read_b32 v173, v172\n" \
    "s_waitcnt lgkmcnt(0)\n" \
    "v_cmp_gt_u32 vcc, 1, v173\n" \
    "s_nop 1\n" \
    "s_cbranch_vccnz Lw2%=\n" \
    "s_branch Lr2%=\n" \
    "Lh2m%=:\n" \
    "v_mov_b32 v172, 148744\n" \
    "s_branch Lw6e%=\n" \
    "Lw6%=:\n" \
    "s_sleep 1\n" \
    "Lw6e%=:\n" \
    "ds_read_b32 v173, v172\n" \
    "s_waitcnt lgkmcnt(0)\n" \
    "v_cmp_gt_u32 vcc, 1, v173\n" \
    "s_nop 1\n" \
    "s_cbranch_vccnz Lw6%=\n" \
    "s_branch Lr2m%=\n" \
    "Lmt%=:\n" \
    "s_nop 7\n" \
    "v_lshrrev_b32 v0, 2, v43\n" \
    "v_bfe_u32 v1, v39, 8, 2\n" \
    "v_lshlrev_b32 v1, 3, v1\n" \
    "v_mul_u32_u24 v0, 98, v0\n" \
    "v_add_u32 v2, v0, v1\n" \
    "v_add_u32 v3, 4704, v1\n" \
    "v_mov_b32 v4, 4800\n" \
    "v_add_u32 v53, 148992, v39\n" \
    "s_mov_b32 s64, 0x3fb8aa3b\n" \
    "buffer_load_dwordx2 v[6:7], v2, s[44:47], s57 offen\n" \
    "buffer_load_dwordx2 v[8:9], v2, s[44:47], s57 offen offset:32\n" \
    "buffer_load_dwordx2 v[10:11], v2, s[44:47], s57 offen offset:64\n" \
    "buffer_load_dwordx2 v[12:13], v2, s[44:47], s57 offen offset:1568\n" \
    "buffer_load_dwordx2 v[14:15], v2, s[44:47], s57 offen offset:1600\n" \
    "buffer_load_dwordx2 v[16:17], v2, s[44:47], s57 offen offset:1632\n" \
    "buffer_load_dwordx2 v[18:19], v2, s[44:47], s57 offen offset:3136\n" \
    "buffer_load_dwordx2 v[20:21], v2, s[44:47], s57 offen offset:3168\n" \
    "buffer_load_dwordx2 v[22:23], v2, s[44:47], s57 offen offset:3200\n" \
    "buffer_load_dwordx2 v[24:25], v3, s[44:47], s57 offen\n" \
    "buffer_load_dwordx2 v[26:27], v3, s[44:47], s57 offen offset:32\n" \
    "buffer_load_dwordx2 v[28:29], v3, s[44:47], s57 offen offset:64\n" \
    "buffer_load_ushort v30, v0, s[44:47], s57 offen offset:96\n" \
    "buffer_load_ushort v31, v0, s[44:47], s57 offen offset:1664\n" \
    "buffer_load_ushort v32, v0, s[44:47], s57 offen offset:3232\n" \
    "buffer_load_ushort v33, v4, s[44:47], s57 offen\n" \
    "v_mov_b32 v34, 148740\n" \
    "s_branch Lw5e%=\n" \
    "Lw5%=:\n" \
    "s_sleep 1\n" \
    "Lw5e%=:\n" \
    "ds_read_b32 v35, v34\n" \
    "s_waitcnt lgkmcnt(0)\n" \
    "v_cmp_gt_u32 vcc, 4, v35\n" \
    "s_nop 1\n" \
    "s_cbranch_vccnz Lw5%=\n" \
    "s_waitcnt vmcnt(0)\n" \
    "v_lshlrev_b32 v204, 16, v6\n" \
    "v_and_b32 v205, 0xffff0000, v6\n" \
    "v_lshlrev_b32 v206, 16, v7\n" \
    "v_and_b32 v207, 0xffff0000, v7\n" \
    "v_mul_f32 v204, s64, v204\n" \
    "v_mul_f32 v205, s64, v205\n" \
    "v_mul_f32 v206, s64, v206\n" \
    "v_mul_f32 v207, s64, v207\n" \
    "ds_write_b128 v53, v[204:207]\n" \
    "v_lshlrev_b32 v208, 16, v8\n" \
    "v_and_b32 v209, 0xffff0000, v8\n" \
    "v_lshlrev_b32 v210, 16, v9\n" \
    "v_and_b32 v211, 0xffff0000, v9\n" \
    "v_mul_f32 v208, s64, v208\n" \
    "v_mul_f32 v209, s64, v209\n" \
    "v_mul_f32 v210, s64, v210\n" \
    "v_mul_f32 v211, s64, v211\n" \
    "ds_write_b128 v53, v[208:211] offset:1024\n" \
    "v_lshlrev_b32 v212, 16, v10\n" \
    "v_and_b32 v213, 0xffff0000, v10\n" \
    "v_lshlrev_b32 v214, 16, v11\n" \
    "v_and_b32 v215, 0xffff0000, v11\n" \
    "v_mul_f32 v212, s64, v212\n" \
    "v_mul_f32 v213, s64, v213\n" \
    "v_mul_f32 v214, s64, v214\n" \
    "v_mul_f32 v215, s64, v215\n" \
    "ds_write_b128 v53, v[212:215] offset:2048\n" \
    "v_lshlrev_b32 v216, 16, v12\n" \
    "v_and_b32 v217, 0xffff0000, v12\n" \
    "v_lshlrev_b32 v218, 16, v13\n" \
    "v_and_b32 v219, 0xffff0000, v13\n" \
    "v_mul_f32 v216, s64, v216\n" \
    "v_mul_f32 v217, s64, v217\n" \
    "v_mul_f32 v218, s64, v218\n" \
    "v_mul_f32 v219, s64, v219\n" \
    "ds_write_b128 v53, v[216:219] offset:3072\n" \
    "v_lshlrev_b32 v220, 16, v14\n" \
    "v_and_b32 v221, 0xffff0000, v14\n" \
    "v_lshlrev_b32 v222, 16, v15\n" \
    "v_and_b32 v223, 0xffff0000, v15\n" \
    "v_mul_f32 v220, s64, v220\n" \
    "v_mul_f32 v221, s64, v221\n" \
    "v_mul_f32 v222, s64, v222\n" \
    "v_mul_f32 v223, s64, v223\n" \
    "ds_write_b128 v53, v[220:223] offset:4096\n" \
    "v_lshlrev_b32 v224, 16, v16\n" \
    "v_and_b32 v225, 0xffff0000, v16\n" \
    "v_lshlrev_b32 v226, 16, v17\n" \
    "v_and_b32 v227, 0xffff0000, v17\n" \
    "v_mul_f32 v224, s64, v224\n" \
    "v_mul_f32 v225, s64, v225\n" \
    "v_mul_f32 v226, s64, v226\n" \
    "v_mul_f32 v227, s64, v227\n" \
    "ds_write_b128 v53, v[224:227] offset:5120\n" \
    "v_lshlrev_b32 v228, 16, v18\n" \
    "v_and_b32 v229, 0xffff0000, v18\n" \
    "v_lshlrev_b32 v230, 16, v19\n" \
    "v_and_b32 v231, 0xffff0000, v19\n" \
    "v_mul_f32 v228, s64, v228\n" \
    "v_mul_f32 v229, s64, v229\n" \
    "v_mul_f32 v230, s64, v230\n" \
    "v_mul_f32 v231, s64, v231\n" \
    "ds_write_b128 v53, v[228:231] offset:6144\n" \
    "v_lshlrev_b32 v232, 16, v20\n" \
    "v_and_b32 v233, 0xffff0000, v20\n" \
    "v_lshlrev_b32 v234, 16, v21\n" \
    "v_and_b32 v235, 0xffff0000, v21\n" \
    "v_mul_f32 v232, s64, v232\n" \
    "v_mul_f32 v233, s64, v233\n" \
    "v_mul_f32 v234, s64, v234\n" \
    "v_mul_f32 v235, s64, v235\n" \
    "ds_write_b128 v53, v[232:235] offset:7168\n" \
    "v_lshlrev_b32 v236, 16, v22\n" \
    "v_and_b32 v237, 0xffff0000, v22\n" \
    "v_lshlrev_b32 v238, 16, v23\n" \
    "v_and_b32 v239, 0xffff0000, v23\n" \
    "v_mul_f32 v236, s64, v236\n" \
    "v_mul_f32 v237, s64, v237\n" \
    "v_mul_f32 v238, s64, v238\n" \
    "v_mul_f32 v239, s64, v239\n" \
    "ds_write_b128 v53, v[236:239] offset:8192\n" \
    "v_lshlrev_b32 v240, 16, v24\n" \
    "v_and_b32 v241, 0xffff0000, v24\n" \
    "v_lshlrev_b32 v242, 16, v25\n" \
    "v_and_b32 v243, 0xffff0000, v25\n" \
    "v_mul_f32 v240, s64, v240\n" \
    "v_mul_f32 v241, s64, v241\n" \
    "v_mul_f32 v242, s64, v242\n" \
    "v_mul_f32 v243, s64, v243\n" \
    "ds_write_b128 v53, v[240:243] offset:9216\n" \
    "v_lshlrev_b32 v244, 16, v26\n" \
    "v_and_b32 v245, 0xffff0000, v26\n" \
    "v_lshlrev_b32 v246, 16, v27\n" \
    "v_and_b32 v247, 0xffff0000, v27\n" \
    "v_mul_f32 v244, s64, v244\n" \
    "v_mul_f32 v245, s64, v245\n" \
    "v_mul_f32 v246, s64, v246\n" \
    "v_mul_f32 v247, s64, v247\n" \
    "ds_write_b128 v53, v[244:247] offset:10240\n" \
    "v_lshlrev_b32 v248, 16, v28\n" \
    "v_and_b32 v249, 0xffff0000, v28\n" \
    "v_lshlrev_b32 v250, 16, v29\n" \
    "v_and_b32 v251, 0xffff0000, v29\n" \
    "v_mul_f32 v248, s64, v248\n" \
    "v_mul_f32 v249, s64, v249\n" \
    "v_mul_f32 v250, s64, v250\n" \
    "v_mul_f32 v251, s64, v251\n" \
    "ds_write_b128 v53, v[248:251] offset:11264\n" \
    "v_lshlrev_b32 v252, 16, v30\n" \
    "v_lshlrev_b32 v253, 16, v31\n" \
    "v_lshlrev_b32 v254, 16, v32\n" \
    "v_lshlrev_b32 v255, 16, v33\n" \
    "v_mul_f32 v252, s64, v252\n" \
    "v_mul_f32 v253, s64, v253\n" \
    "v_mul_f32 v254, s64, v254\n" \
    "v_mul_f32 v255, s64, v255\n" \
    "ds_write_b128 v53, v[252:255] offset:12288\n" \
    "s_waitcnt lgkmcnt(0)\n" \
    "s_branch Lr1m%=\n" \
    "Lalt%=:\n" \
    "s_sub_u32 s60, s54, 1\n" \
    "s_or_b32 s60, s60, s75\n" \
    "s_cmp_lg_u32 s60, 0\n" \
    "s_cbranch_scc1 Lexit%=\n" \
    "s_waitcnt vmcnt(0) lgkmcnt(0)\n" \
    "s_nop 7\n" \
    "s_sub_u32 s60, s52, 2809\n" \
    "s_mul_i32 s61, s52, 19785\n" \
    "s_lshr_b32 s61, s61, 20\n" \
    "s_mul_i32 s62, s61, 53\n" \
    "s_sub_u32 s62, s52, s62\n" \
    "s_sub_u32 s63, s60, 54\n" \
    "s_cmp_lt_i32 s60, 54\n" \
    "s_cselect_b32 s63, 53, s63\n" \
    "s_cselect_b32 s64, s60, 53\n" \
    "s_cmp_lt_i32 s60, 0\n" \
    "s_cselect_b32 s61, s61, s63\n" \
    "s_cselect_b32 s62, s62, s64\n" \
    "s_mul_i32 s57, s61, 54\n" \
    "s_add_u32 s57, s57, s62\n" \
    "s_mul_i32 s57, s57, 4802\n" \
    "s_mul_i32 s61, s61, 7\n" \
    "s_add_u32 s61, s61, 3\n" \
    "s_mul_i32 s62, s62, 7\n" \
    "s_add_u32 s62, s62, 3\n" \
    "s_mov_b32 s56, 0\n" \
    "s_mov_b32 s42, 27433728\n" \
    "s_mov_b32 s46, 14002636\n" \
    "s_mov_b32 s63, 72576\n" \
    "s_movk_i32 s64, 192\n" \
    "v_lshrrev_b32 v56, 2, v43\n" \
    "v_bfe_u32 v57, v39, 8, 2\n" \
    "v_lshlrev_b32 v57, 4, v57\n" \
    "v_add_u32 v58, 0, v56\n" \
    "v_mul_u32_u24 v59, 37, v58\n" \
    "v_lshrrev_b32 v59, 8, v59\n" \
    "v_mul_u32_u24 v60, 7, v59\n" \
    "v_sub_u32 v58, v58, v60\n" \
    "v_add_u32 v59, s61, v59\n" \
    "v_add_u32 v58, s62, v58\n" \
    "v_subrev_u32 v60, 378, v59\n" \
    "v_min_u32 v59, v59, v60\n" \
    "v_subrev_u32 v60, 378, v58\n" \
    "v_min_u32 v58, v58, v60\n" \
    "v_mad_u32_u24 v58, v58, s64, v57\n" \
    "v_mad_u32_u24 v36, v59, s63, v58\n" \
    "buffer_load_dwordx4 v[0:3], v36, s[40:43], 0 offen\n" \
    "buffer_load_dwordx4 v[4:7], v36, s[40:43], 0 offen offset:64\n" \
    "buffer_load_dwordx4 v[8:11], v36, s[40:43], 0 offen offset:128\n" \
    "v_add_u32 v58, 16, v56\n" \
    "v_mul_u32_u24 v59, 37, v58\n" \
    "v_lshrrev_b32 v59, 8, v59\n" \
    "v_mul_u32_u24 v60, 7, v59\n" \
    "v_sub_u32 v58, v58, v60\n" \
    "v_add_u32 v59, s61, v59\n" \
    "v_add_u32 v58, s62, v58\n" \
    "v_subrev_u32 v60, 378, v59\n" \
    "v_min_u32 v59, v59, v60\n" \
    "v_subrev_u32 v60, 378, v58\n" \
    "v_min_u32 v58, v58, v60\n" \
    "v_mad_u32_u24 v58, v58, s64, v57\n" \
    "v_mad_u32_u24 v37, v59, s63, v58\n" \
    "buffer_load_dwordx4 v[12:15], v37, s[40:43], 0 offen\n" \
    "buffer_load_dwordx4 v[16:19], v37, s[40:43], 0 offen offset:64\n" \
    "buffer_load_dwordx4 v[20:23], v37, s[40:43], 0 offen offset:128\n" \
    "v_add_u32 v58, 32, v56\n" \
    "v_mul_u32_u24 v59, 37, v58\n" \
    "v_lshrrev_b32 v59, 8, v59\n" \
    "v_mul_u32_u24 v60, 7, v59\n" \
    "v_sub_u32 v58, v58, v60\n" \
    "v_add_u32 v59, s61, v59\n" \
    "v_add_u32 v58, s62, v58\n" \
    "v_subrev_u32 v60, 378, v59\n" \
    "v_min_u32 v59, v59, v60\n" \
    "v_subrev_u32 v60, 378, v58\n" \
    "v_min_u32 v58, v58, v60\n" \
    "v_mad_u32_u24 v58, v58, s64, v57\n" \
    "v_mad_u32_u24 v38, v59, s63, v58\n" \
    "buffer_load_dwordx4 v[24:27], v38, s[40:43], 0 offen\n" \
    "buffer_load_dwordx4 v[28:31], v38, s[40:43], 0 offen offset:64\n" \
    "buffer_load_dwordx4 v[32:35], v38, s[40:43], 0 offen offset:128\n" \
    "s_mov_b32 s60, 0x12800000\n" \
    "s_mov_b32 s62, 0x5d800000\n" \
    "s_waitcnt vmcnt(0)\n" \
    "ds_read_b128 v[228:231], v39\n" \
    "ds_read_b128 v[232:235], v39 offset:1040\n" \
    "ds_read_b128 v[236:239], v39 offset:2080\n" \
    "ds_read_b128 v[176:179], v39 offset:3120\n" \
    "ds_read_b128 v[180:183], v39 offset:4160\n" \
    "ds_read_b128 v[184:187], v39 offset:5200\n" \
    "s_waitcnt lgkmcnt(5)\n" \
    "v_mfma_f32_16x16x32_bf16 v[204:207], v[228:231], v[0:3], 0\n" \
    "v_mfma_f32_16x16x32_bf16 v[208:211], v[228:231], v[12:15], 0\n" \
    "v_mfma_f32_16x16x32_bf16 v[212:215], v[228:231], v[24:27], 0\n" \
    "ds_read_b128 v[228:231], v39 offset:6240\n" \
    "s_waitcnt lgkmcnt(1)\n" \
    "v_mfma_f32_16x16x32_bf16 v[204:207], v[232:235], v[4:7], v[204:207]\n" \
    "v_mfma_f32_16x16x32_bf16 v[208:211], v[232:235], v[16:19], v[208:211]\n" \
    "v_mfma_f32_16x16x32_bf16 v[212:215], v[232:235], v[28:31], v[212:215]\n" \
    "ds_read_b128 v[232:235], v39 offset:7280\n" \
    "v_mfma_f32_16x16x32_bf16 v[204:207], v[236:239], v[8:11], v[204:207]\n" \
    "v_mfma_f32_16x16x32_bf16 v[208:211], v[236:239], v[20:23], v[208:211]\n" \
    "v_mfma_f32_16x16x32_bf16 v[212:215], v[236:239], v[32:35], v[212:215]\n" \
    "ds_read_b128 v[236:239], v39 offset:8320\n" \
    "v_mov_b32 v55, s70\n" \
    "v_mfma_f32_16x16x32_bf16 v[216:219], v[176:179], v[0:3], 0\n" \
    "v_mfma_f32_16x16x32_bf16 v[220:223], v[176:179], v[12:15], 0\n" \
    "s_nop 1\n" \
    "v_cvt_pk_f16_f32 v56, v204, v205\n" \
    "v_mfma_f32_16x16x32_bf16 v[224:227], v[176:179], v[24:27], 0\n" \
    "ds_read_b128 v[176:179], v39 offset:9360\n" \
    "v_pk_mul_f16 v240, v56, v56\n" \
    "v_mfma_f32_16x16x32_bf16 v[216:219], v[180:183], v[4:7], v[216:219]\n" \
    "v_cvt_pk_f16_f32 v57, v206, v207\n" \
    "v_pk_fma_f16 v240, v57, v57, v240\n" \
    "v_mfma_f32_16x16x32_bf16 v[220:223], v[180:183], v[16:19], v[220:223]\n" \
    "v_cvt_pk_f16_f32 v68, v208, v209\n" \
    "v_pk_mul_f16 v244, v68, v68\n" \
    "v_mfma_f32_16x16x32_bf16 v[224:227], v[180:183], v[28:31], v[224:227]\n" \
    "ds_read_b128 v[180:183], v39 offset:10400\n" \
    "v_cvt_pk_f16_f32 v69, v210, v211\n" \
    "v_mfma_f32_16x16x32_bf16 v[216:219], v[184:187], v[8:11], v[216:219]\n" \
    "v_pk_fma_f16 v244, v69, v69, v244\n" \
    "v_cvt_pk_f16_f32 v80, v212, v213\n" \
    "v_mfma_f32_16x16x32_bf16 v[220:223], v[184:187], v[20:23], v[220:223]\n" \
    "v_pk_mul_f16 v248, v80, v80\n" \
    "v_cvt_pk_f16_f32 v81, v214, v215\n" \
    "v_mfma_f32_16x16x32_bf16 v[224:227], v[184:187], v[32:35], v[224:227]\n" \
    "ds_read_b128 v[184:187], v39 offset:11440\n" \
    "v_pk_fma_f16 v248, v81, v81, v248\n" \
    "s_waitcnt lgkmcnt(3)\n" \
    "v_mfma_f32_16x16x32_bf16 v[204:207], v[228:231], v[0:3], 0\n" \
    "v_cvt_pk_f16_f32 v58, v216, v217\n" \
    "v_pk_fma_f16 v240, v58, v58, v240\n" \
    "v_mfma_f32_16x16x32_bf16 v[208:211], v[228:231], v[12:15], 0\n" \
    "v_cvt_pk_f16_f32 v59, v218, v219\n" \
    "v_pk_mul_f16 v241, v59, v59\n" \
    "v_mfma_f32_16x16x32_bf16 v[212:215], v[228:231], v[24:27], 0\n" \
    "ds_read_b128 v[228:231], v39 offset:12480\n" \
    "v_cvt_pk_f16_f32 v70, v220, v221\n" \
    "v_mfma_f32_16x16x32_bf16 v[204:207], v[232:235], v[4:7], v[204:207]\n" \
    "v_pk_fma_f16 v244, v70, v70, v244\n" \
    "v_cvt_pk_f16_f32 v71, v222, v223\n" \
    "v_mfma_f32_16x16x32_bf16 v[208:211], v[232:235], v[16:19], v[208:211]\n" \
    "v_pk_mul_f16 v245, v71, v71\n" \
    "v_cvt_pk_f16_f32 v82, v224, v225\n" \
    "v_mfma_f32_16x16x32_bf16 v[212:215], v[232:235], v[28:31], v[212:215]\n" \
    "ds_read_b128 v[232:235], v39 offset:13520\n" \
    "v_pk_fma_f16 v248, v82, v82, v248\n" \
    "v_mfma_f32_16x16x32_bf16 v[204:207], v[236:239], v[8:11], v[204:207]\n" \
    "v_cvt_pk_f16_f32 v83, v226, v227\n" \
    "v_pk_mul_f16 v249, v83, v83\n" \
    "v_mfma_f32_16x16x32_bf16 v[208:211], v[236:239], v[20:23], v[208:211]\n" \
    "v_mfma_f32_16x16x32_bf16 v[212:215], v[236:239], v[32:35], v[212:215]\n" \
    "ds_read_b128 v[236:239], v39 offset:14560\n" \
    "s_waitcnt lgkmcnt(3)\n" \
    "v_mfma_f32_16x16x32_bf16 v[216:219], v[176:179], v[0:3], 0\n" \
    "s_nop 0\n" \
    "v_cvt_pk_f16_f32 v60, v204, v205\n" \
    "v_mfma_f32_16x16x32_bf16 v[220:223], v[176:179], v[12:15], 0\n" \
    "v_pk_fma_f16 v241, v60, v60, v241\n" \
    "v_cvt_pk_f16_f32 v61, v206, v207\n" \
    "v_mfma_f32_16x16x32_bf16 v[224:227], v[176:179], v[24:27], 0\n" \
    "ds_read_b128 v[176:179], v39 offset:15600\n" \
    "v_pk_fma_f16 v241, v61, v61, v241\n" \
    "v_mfma_f32_16x16x32_bf16 v[216:219], v[180:183], v[4:7], v[216:219]\n" \
    "v_cvt_pk_f16_f32 v72, v208, v209\n" \
    "v_pk_fma_f16 v245, v72, v72, v245\n" \
    "v_mfma_f32_16x16x32_bf16 v[220:223], v[180:183], v[16:19], v[220:223]\n" \
    "v_cvt_pk_f16_f32 v73, v210, v211\n" \
    "v_pk_fma_f16 v245, v73, v73, v245\n" \
    "v_mfma_f32_16x16x32_bf16 v[224:227], v[180:183], v[28:31], v[224:227]\n" \
    "ds_read_b128 v[180:183], v39 offset:16640\n" \
    "v_cvt_pk_f16_f32 v84, v212, v213\n" \
    "v_mfma_f32_16x16x32_bf16 v[216:219], v[184:187], v[8:11], v[216:219]\n" \
    "v_pk_fma_f16 v249, v84, v84, v249\n" \
    "v_cvt_pk_f16_f32 v85, v214, v215\n" \
    "v_mfma_f32_16x16x32_bf16 v[220:223], v[184:187], v[20:23], v[220:223]\n" \
    "v_pk_fma_f16 v249, v85, v85, v249\n" \
    "v_mfma_f32_16x16x32_bf16 v[224:227], v[184:187], v[32:35], v[224:227]\n" \
    "ds_read_b128 v[184:187], v39 offset:17680\n" \
    "s_waitcnt lgkmcnt(3)\n" \
    "v_mfma_f32_16x16x32_bf16 v[204:207], v[228:231], v[0:3], 0\n" \
    "v_cvt_pk_f16_f32 v62, v216, v217\n" \
    "v_pk_mul_f16 v242, v62, v62\n" \
    "v_mfma_f32_16x16x32_bf16 v[208:211], v[228:231], v[12:15], 0\n" \
    "v_cvt_pk_f16_f32 v63, v218, v219\n" \
    "v_pk_fma_f16 v242, v63, v63, v242\n" \
    "v_mfma_f32_16x16x32_bf16 v[212:215], v[228:231], v[24:27], 0\n" \
    "ds_read_b128 v[228:231], v39 offset:18720\n" \
    "v_cvt_pk_f16_f32 v74, v220, v221\n" \
    "v_mfma_f32_16x16x32_bf16 v[204:207], v[232:235], v[4:7], v[204:207]\n" \
    "v_pk_mul_f16 v246, v74, v74\n" \
    "v_cvt_pk_f16_f32 v75, v222, v223\n" \
    "v_mfma_f32_16x16x32_bf16 v[208:211], v[232:235], v[16:19], v[208:211]\n" \
    "v_pk_fma_f16 v246, v75, v75, v246\n" \
    "v_cvt_pk_f16_f32 v86, v224, v225\n" \
    "v_mfma_f32_16x16x32_bf16 v[212:215], v[232:235], v[28:31], v[212:215]\n" \
    "ds_read_b128 v[232:235], v39 offset:19760\n" \
    "v_pk_mul_f16 v250, v86, v86\n" \
    "v_mfma_f32_16x16x32_bf16 v[204:207], v[236:239], v[8:11], v[204:207]\n" \
    "v_cvt_pk_f16_f32 v87, v226, v227\n" \
    "v_pk_fma_f16 v250, v87, v87, v250\n" \
    "v_mfma_f32_16x16x32_bf16 v[208:211], v[236:239], v[20:23], v[208:211]\n" \
    "v_mfma_f32_16x16x32_bf16 v[212:215], v[236:239], v[32:35], v[212:215]\n" \
    "ds_read_b128 v[236:239], v39 offset:20800\n" \
    "s_waitcnt lgkmcnt(3)\n" \
    "v_mfma_f32_16x16x32_bf16 v[216:219], v[176:179], v[0:3], 0\n" \
    "s_nop 0\n" \
    "v_cvt_pk_f16_f32 v64, v204, v205\n" \
    "v_mfma_f32_16x16x32_bf16 v[220:223], v[176:179], v[12:15], 0\n" \
    "v_pk_fma_f16 v242, v64, v64, v242\n" \
    "v_cvt_pk_f16_f32 v65, v206, v207\n" \
    "v_mfma_f32_16x16x32_bf16 v[224:227], v[176:179], v[24:27], 0\n" \
    "ds_read_b128 v[176:179], v39 offset:21840\n" \
    "v_pk_mul_f16 v243, v65, v65\n" \
    "v_mfma_f32_16x16x32_bf16 v[216:219], v[180:183], v[4:7], v[216:219]\n" \
    "v_cvt_pk_f16_f32 v76, v208, v209\n" \
    "v_pk_fma_f16 v246, v76, v76, v246\n" \
    "v_mfma_f32_16x16x32_bf16 v[220:223], v[180:183], v[16:19], v[220:223]\n" \
    "v_cvt_pk_f16_f32 v77, v210, v211\n" \
    "v_pk_mul_f16 v247, v77, v77\n" \
    "v_mfma_f32_16x16x32_bf16 v[224:227], v[180:183], v[28:31], v[224:227]\n" \
    "ds_read_b128 v[180:183], v39 offset:22880\n" \
    "v_cvt_pk_f16_f32 v88, v212, v213\n" \
    "v_mfma_f32_16x16x32_bf16 v[216:219], v[184:187], v[8:11], v[216:219]\n" \
    "v_pk_fma_f16 v250, v88, v88, v250\n" \
    "v_cvt_pk_f16_f32 v89, v214, v215\n" \
    "v_mfma_f32_16x16x32_bf16 v[220:223], v[184:187], v[20:23], v[220:223]\n" \
    "v_pk_mul_f16 v251, v89, v89\n" \
    "v_mfma_f32_16x16x32_bf16 v[224:227], v[184:187], v[32:35], v[224:227]\n" \
    "ds_read_b128 v[184:187], v39 offset:23920\n" \
    "s_waitcnt lgkmcnt(3)\n" \
    "v_mfma_f32_16x16x32_bf16 v[204:207], v[228:231], v[0:3], 0\n" \
    "v_cvt_pk_f16_f32 v66, v216, v217\n" \
    "v_pk_fma_f16 v243, v66, v66, v243\n" \
    "v_mfma_f32_16x16x32_bf16 v[208:211], v[228:231], v[12:15], 0\n" \
    "v_cvt_pk_f16_f32 v67, v218, v219\n" \
    "v_pk_fma_f16 v243, v67, v67, v243\n" \
    "v_mfma_f32_16x16x32_bf16 v[212:215], v[228:231], v[24:27], 0\n" \
    "ds_read_b128 v[228:231], v39 offset:24960\n" \
    "v_cvt_pk_f16_f32 v78, v220, v221\n" \
    "v_mfma_f32_16x16x32_f16 v[140:143], v[48:51], v[240:243], 0\n" \
    "v_pk_fma_f16 v247, v78, v78, v247\n" \
    "v_cvt_pk_f16_f32 v79, v222, v223\n" \
    "v_mfma_f32_16x16x32_bf16 v[204:207], v[232:235], v[4:7], v[204:207]\n" \
    "v_pk_fma_f16 v247, v79, v79, v247\n" \
    "v_cvt_pk_f16_f32 v90, v224, v225\n" \
    "v_mfma_f32_16x16x32_bf16 v[208:211], v[232:235], v[16:19], v[208:211]\n" \
    "v_pk_fma_f16 v251, v90, v90, v251\n" \
    "v_cvt_pk_f16_f32 v91, v226, v227\n" \
    "v_mfma_f32_16x16x32_f16 v[144:147], v[48:51], v[244:247], 0\n" \
    "v_pk_fma_f16 v251, v91, v91, v251\n" \
    "v_rsq_f32 v141, v140\n" \
    "v_mfma_f32_16x16x32_bf16 v[212:215], v[232:235], v[28:31], v[212:215]\n" \
    "ds_read_b128 v[232:235], v39 offset:26000\n" \
    "v_cvt_f16_f32 v141, v141\n" \
    "v_mfma_f32_16x16x32_f16 v[148:151], v[48:51], v[248:251], 0\n" \
    "ds_bpermute_b32 v164, v43, v141\n" \
    "ds_bpermute_b32 v165, v43, v141 offset:64\n" \
    "v_mfma_f32_16x16x32_bf16 v[204:207], v[236:239], v[8:11], v[204:207]\n" \
    "ds_bpermute_b32 v166, v43, v141 offset:128\n" \
    "ds_bpermute_b32 v167, v43, v141 offset:192\n" \
    "v_mfma_f32_16x16x32_bf16 v[208:211], v[236:239], v[20:23], v[208:211]\n" \
    "v_rsq_f32 v145, v144\n" \
    "v_mfma_f32_16x16x32_bf16 v[212:215], v[236:239], v[32:35], v[212:215]\n" \
    "ds_read_b128 v[236:239], v39 offset:27040\n" \
    "v_cvt_f16_f32 v145, v145\n" \
    "s_waitcnt lgkmcnt(6)\n" \
    "v_mfma_f32_16x16x32_bf16 v[216:219], v[176:179], v[0:3], 0\n" \
    "ds_bpermute_b32 v168, v43, v145\n" \
    "ds_bpermute_b32 v169, v43, v145 offset:64\n" \
    "v_mfma_f32_16x16x32_bf16 v[220:223], v[176:179], v[12:15], 0\n" \
    "ds_bpermute_b32 v170, v43, v145 offset:128\n" \
    "ds_bpermute_b32 v171, v43, v145 offset:192\n" \
    "v_mfma_f32_16x16x32_bf16 v[224:227], v[176:179], v[24:27], 0\n" \
    "ds_read_b128 v[176:179], v39 offset:28080\n" \
    "v_rsq_f32 v149, v148\n" \
    "v_mfma_f32_16x16x32_bf16 v[216:219], v[180:183], v[4:7], v[216:219]\n" \
    "v_cvt_f16_f32 v149, v149\n" \
    "s_waitcnt lgkmcnt(8)\n" \
    "v_pk_mul_f16 v56, v56, v164 op_sel_hi:[1,0]\n" \
    "v_mfma_f32_16x16x32_bf16 v[220:223], v[180:183], v[16:19], v[220:223]\n" \
    "ds_bpermute_b32 v172, v43, v149\n" \
    "ds_bpermute_b32 v173, v43, v149 offset:64\n" \
    "v_mfma_f32_16x16x32_bf16 v[224:227], v[180:183], v[28:31], v[224:227]\n" \
    "ds_bpermute_b32 v174, v43, v149 offset:128\n" \
    "ds_bpermute_b32 v175, v43, v149 offset:192\n" \
    "v_mfma_f32_16x16x32_bf16 v[216:219], v[184:187], v[8:11], v[216:219]\n" \
    "ds_read_b128 v[180:183], v39 offset:29120\n" \
    "v_pk_mul_f16 v57, v57, v164 op_sel_hi:[1,0]\n" \
    "v_mfma_f32_16x16x32_bf16 v[220:223], v[184:187], v[20:23], v[220:223]\n" \
    "v_pk_mul_f16 v58, v58, v164 op_sel_hi:[1,0]\n" \
    "v_pk_mul_f16 v59, v59, v165 op_sel_hi:[1,0]\n" \
    "v_mfma_f32_16x16x32_bf16 v[224:227], v[184:187], v[32:35], v[224:227]\n" \
    "v_pk_mul_f16 v60, v60, v165 op_sel_hi:[1,0]\n" \
    "v_pk_mul_f16 v61, v61, v165 op_sel_hi:[1,0]\n" \
    "s_waitcnt lgkmcnt(5)\n" \
    "v_pk_mul_f16 v62, v62, v166 op_sel_hi:[1,0]\n" \
    "v_pk_mul_f16 v63, v63, v166 op_sel_hi:[1,0]\n" \
    "v_pk_mul_f16 v64, v64, v166 op_sel_hi:[1,0]\n" \
    "v_pk_mul_f16 v65, v65, v167 op_sel_hi:[1,0]\n" \
    "v_pk_mul_f16 v66, v66, v167 op_sel_hi:[1,0]\n" \
    "v_pk_mul_f16 v67, v67, v167 op_sel_hi:[1,0]\n" \
    "v_pk_mul_f16 v68, v68, v168 op_sel_hi:[1,0]\n" \
    "v_pk_mul_f16 v69, v69, v168 op_sel_hi:[1,0]\n" \
    "v_pk_mul_f16 v70, v70, v168 op_sel_hi:[1,0]\n" \
    "v_cvt_pk_f16_f32 v92, v204, v205\n" \
    "v_pk_mul_f16 v71, v71, v169 op_sel_hi:[1,0]\n" \
    "v_pk_mul_f16 v240, v92, v92\n" \
    "v_pk_mul_f16 v72, v72, v169 op_sel_hi:[1,0]\n" \
    "v_cvt_pk_f16_f32 v93, v206, v207\n" \
    "v_mfma_f32_16x16x32_bf16 v[204:207], v[228:231], v[0:3], 0\n" \
    "ds_read_b128 v[184:187], v39 offset:30160\n" \
    "v_pk_mul_f16 v73, v73, v169 op_sel_hi:[1,0]\n" \
    "v_mfma_f32_16x16x32_bf16 v[204:207], v[232:235], v[4:7], v[204:207]\n" \
    "v_pk_fma_f16 v240, v93, v93, v240\n" \
    "v_pk_mul_f16 v74, v74, v170 op_sel_hi:[1,0]\n" \
    "v_mfma_f32_16x16x32_bf16 v[204:207], v[236:239], v[8:11], v[204:207]\n" \
    "v_cvt_pk_f16_f32 v108, v208, v209\n" \
    "v_pk_mul_f16 v75, v75, v170 op_sel_hi:[1,0]\n" \
    "v_pk_mul_f16 v244, v108, v108\n" \
    "v_pk_mul_f16 v76, v76, v170 op_sel_hi:[1,0]\n" \
    "v_cvt_pk_f16_f32 v109, v210, v211\n" \
    "v_mfma_f32_16x16x32_bf16 v[208:211], v[228:231], v[12:15], 0\n" \
    "v_pk_mul_f16 v77, v77, v171 op_sel_hi:[1,0]\n" \
    "v_pk_fma_f16 v244, v109, v109, v244\n" \
    "v_mfma_f32_16x16x32_bf16 v[208:211], v[232:235], v[16:19], v[208:211]\n" \
    "v_pk_mul_f16 v78, v78, v171 op_sel_hi:[1,0]\n" \
    "v_cvt_pk_f16_f32 v124, v212, v213\n" \
    "v_mfma_f32_16x16x32_bf16 v[208:211], v[236:239], v[20:23], v[208:211]\n" \
    "v_pk_mul_f16 v79, v79, v171 op_sel_hi:[1,0]\n" \
    "v_pk_mul_f16 v248, v124, v124\n" \
    "s_waitcnt lgkmcnt(1)\n" \
    "v_pk_mul_f16 v80, v80, v172 op_sel_hi:[1,0]\n" \
    "v_cvt_pk_f16_f32 v125, v214, v215\n" \
    "v_mfma_f32_16x16x32_bf16 v[212:215], v[228:231], v[24:27], 0\n" \
    "ds_read_b128 v[228:231], v39 offset:31200\n" \
    "v_pk_mul_f16 v81, v81, v172 op_sel_hi:[1,0]\n" \
    "v_mfma_f32_16x16x32_bf16 v[212:215], v[232:235], v[28:31], v[212:215]\n" \
    "ds_read_b128 v[232:235], v39 offset:32240\n" \
    "v_pk_fma_f16 v248, v125, v125, v248\n" \
    "v_mfma_f32_16x16x32_bf16 v[212:215], v[236:239], v[32:35], v[212:215]\n" \
    "ds_read_b128 v[236:239], v39 offset:33280\n" \
    "v_pk_mul_f16 v82, v82, v172 op_sel_hi:[1,0]\n" \
    "v_pk_mul_f16 v83, v83, v173 op_sel_hi:[1,0]\n" \
    "v_pk_mul_f16 v84, v84, v173 op_sel_hi:[1,0]\n" \
    "v_pk_mul_f16 v85, v85, v173 op_sel_hi:[1,0]\n" \
    "v_pk_mul_f16 v86, v86, v174 op_sel_hi:[1,0]\n" \
    "v_pk_mul_f16 v87, v87, v174 op_sel_hi:[1,0]\n" \
    "v_pk_mul_f16 v88, v88, v174 op_sel_hi:[1,0]\n" \
    "v_pk_mul_f16 v89, v89, v175 op_sel_hi:[1,0]\n" \
    "v_pk_mul_f16 v90, v90, v175 op_sel_hi:[1,0]\n" \
    "v_pk_mul_f16 v91, v91, v175 op_sel_hi:[1,0]\n" \
    "v_cvt_pk_f16_f32 v94, v216, v217\n" \
    "v_pk_fma_f16 v240, v94, v94, v240\n" \
    "v_cvt_pk_f16_f32 v97, v218, v219\n" \
    "v_mfma_f32_16x16x32_bf16 v[216:219], v[176:179], v[0:3], 0\n" \
    "v_pk_mul_f16 v241, v97, v97\n" \
    "v_cvt_pk_f16_f32 v110, v220, v221\n" \
    "v_mfma_f32_16x16x32_bf16 v[216:219], v[180:183], v[4:7], v[216:219]\n" \
    "v_pk_fma_f16 v244, v110, v110, v244\n" \
    "v_cvt_pk_f16_f32 v113, v222, v223\n" \
    "v_mfma_f32_16x16x32_bf16 v[220:223], v[176:179], v[12:15], 0\n" \
    "v_pk_mul_f16 v245, v113, v113\n" \
    "v_cvt_pk_f16_f32 v126, v224, v225\n" \
    "v_mfma_f32_16x16x32_bf16 v[220:223], v[180:183], v[16:19], v[220:223]\n" \
    "v_pk_fma_f16 v248, v126, v126, v248\n" \
    "v_cvt_pk_f16_f32 v129, v226, v227\n" \
    "v_mfma_f32_16x16x32_bf16 v[224:227], v[176:179], v[24:27], 0\n" \
    "ds_read_b128 v[176:179], v39 offset:34320\n" \
    "v_pk_mul_f16 v249, v129, v129\n" \
    "v_mfma_f32_16x16x32_bf16 v[224:227], v[180:183], v[28:31], v[224:227]\n" \
    "ds_read_b128 v[180:183], v39 offset:35360\n" \
    "v_cvt_pk_f16_f32 v98, v204, v205\n" \
    "s_waitcnt lgkmcnt(2)\n" \
    "v_mfma_f32_16x16x32_bf16 v[216:219], v[184:187], v[8:11], v[216:219]\n" \
    "v_pk_fma_f16 v241, v98, v98, v241\n" \
    "v_cvt_pk_f16_f32 v99, v206, v207\n" \
    "v_mfma_f32_16x16x32_bf16 v[220:223], v[184:187], v[20:23], v[220:223]\n" \
    "v_pk_fma_f16 v241, v99, v99, v241\n" \
    "v_cvt_pk_f16_f32 v114, v208, v209\n" \
    "v_mfma_f32_16x16x32_bf16 v[224:227], v[184:187], v[32:35], v[224:227]\n" \
    "ds_read_b128 v[184:187], v39 offset:36400\n" \
    "v_pk_fma_f16 v245, v114, v114, v245\n" \
    "v_mfma_f32_16x16x32_bf16 v[204:207], v[228:231], v[0:3], 0\n" \
    "v_cvt_pk_f16_f32 v115, v210, v211\n" \
    "v_pk_fma_f16 v245, v115, v115, v245\n" \
    "v_mfma_f32_16x16x32_bf16 v[208:211], v[228:231], v[12:15], 0\n" \
    "v_cvt_pk_f16_f32 v130, v212, v213\n" \
    "v_pk_fma_f16 v249, v130, v130, v249\n" \
    "v_mfma_f32_16x16x32_bf16 v[204:207], v[232:235], v[4:7], v[204:207]\n" \
    "v_cvt_pk_f16_f32 v131, v214, v215\n" \
    "v_pk_fma_f16 v249, v131, v131, v249\n" \
    "v_mfma_f32_16x16x32_bf16 v[212:215], v[228:231], v[24:27], 0\n" \
    "ds_read_b128 v[228:231], v39 offset:37440\n" \
    "v_cvt_pk_f16_f32 v100, v216, v217\n" \
    "v_mfma_f32_16x16x32_bf16 v[208:211], v[232:235], v[16:19], v[208:211]\n" \
    "v_pk_mul_f16 v242, v100, v100\n" \
    "v_cvt_pk_f16_f32 v101, v218, v219\n" \
    "v_mfma_f32_16x16x32_bf16 v[212:215], v[232:235], v[28:31], v[212:215]\n" \
    "ds_read_b128 v[232:235], v39 offset:38480\n" \
    "v_pk_fma_f16 v242, v101, v101, v242\n" \
    "v_mfma_f32_16x16x32_bf16 v[204:207], v[236:239], v[8:11], v[204:207]\n" \
    "v_cvt_pk_f16_f32 v116, v220, v221\n" \
    "v_pk_mul_f16 v246, v116, v116\n" \
    "v_mfma_f32_16x16x32_bf16 v[208:211], v[236:239], v[20:23], v[208:211]\n" \
    "v_cvt_pk_f16_f32 v117, v222, v223\n" \
    "v_pk_fma_f16 v246, v117, v117, v246\n" \
    "v_mfma_f32_16x16x32_bf16 v[212:215], v[236:239], v[32:35], v[212:215]\n" \
    "ds_read_b128 v[236:239], v39 offset:39520\n" \
    "v_cvt_pk_f16_f32 v132, v224, v225\n" \
    "s_waitcnt lgkmcnt(3)\n" \
    "v_mfma_f32_16x16x32_bf16 v[216:219], v[176:179], v[0:3], 0\n" \
    "v_pk_mul_f16 v250, v132, v132\n" \
    "v_cvt_pk_f16_f32 v133, v226, v227\n" \
    "v_mfma_f32_16x16x32_bf16 v[220:223], v[176:179], v[12:15], 0\n" \
    "v_pk_fma_f16 v250, v133, v133, v250\n" \
    "v_cvt_pk_f16_f32 v102, v204, v205\n" \
    "v_mfma_f32_16x16x32_bf16 v[224:227], v[176:179], v[24:27], 0\n" \
    "ds_read_b128 v[176:179], v39 offset:40560\n" \
    "v_pk_fma_f16 v242, v102, v102, v242\n" \
    "v_mfma_f32_16x16x32_bf16 v[216:219], v[180:183], v[4:7], v[216:219]\n" \
    "v_cvt_pk_f16_f32 v105, v206, v207\n" \
    "v_pk_mul_f16 v243, v105, v105\n" \
    "v_mfma_f32_16x16x32_bf16 v[220:223], v[180:183], v[16:19], v[220:223]\n" \
    "v_cvt_pk_f16_f32 v118, v208, v209\n" \
    "v_pk_fma_f16 v246, v118, v118, v246\n" \
    "v_mfma_f32_16x16x32_bf16 v[224:227], v[180:183], v[28:31], v[224:227]\n" \
    "ds_read_b128 v[180:183], v39 offset:41600\n" \
    "v_cvt_pk_f16_f32 v121, v210, v211\n" \
    "v_mfma_f32_16x16x32_bf16 v[216:219], v[184:187], v[8:11], v[216:219]\n" \
    "v_pk_mul_f16 v247, v121, v121\n" \
    "v_cvt_pk_f16_f32 v134, v212, v213\n" \
    "v_mfma_f32_16x16x32_bf16 v[220:223], v[184:187], v[20:23], v[220:223]\n" \
    "v_pk_fma_f16 v250, v134, v134, v250\n" \
    "v_cvt_pk_f16_f32 v137, v214, v215\n" \
    "v_mfma_f32_16x16x32_bf16 v[224:227], v[184:187], v[32:35], v[224:227]\n" \
    "ds_read_b128 v[184:187], v39 offset:42640\n" \
    "v_pk_mul_f16 v251, v137, v137\n" \
    "s_waitcnt lgkmcnt(3)\n" \
    "v_mfma_f32_16x16x32_bf16 v[204:207], v[0:3], v[228:231], 0\n" \
    "v_cvt_pk_f16_f32 v106, v216, v217\n" \
    "v_pk_fma_f16 v243, v106, v106, v243\n" \
    "v_mfma_f32_16x16x32_bf16 v[208:211], v[12:15], v[228:231], 0\n" \
    "v_cvt_pk_f16_f32 v107, v218, v219\n" \
    "v_pk_fma_f16 v243, v107, v107, v243\n" \
    "v_mfma_f32_16x16x32_bf16 v[212:215], v[24:27], v[228:231], 0\n" \
    "ds_read_b128 v[228:231], v39 offset:43680\n" \
    "v_cvt_pk_f16_f32 v122, v220, v221\n" \
    "v_mfma_f32_16x16x32_f16 v[152:155], v[48:51], v[240:243], 0\n" \
    "ds_read_b128 v[240:243], v39 offset:46800\n" \
    "v_pk_fma_f16 v247, v122, v122, v247\n" \
    "v_mfma_f32_16x16x32_bf16 v[204:207], v[4:7], v[232:235], v[204:207]\n" \
    "v_cvt_pk_f16_f32 v123, v222, v223\n" \
    "v_pk_fma_f16 v247, v123, v123, v247\n" \
    "v_mfma_f32_16x16x32_bf16 v[208:211], v[16:19], v[232:235], v[208:211]\n" \
    "v_cvt_pk_f16_f32 v138, v224, v225\n" \
    "v_pk_fma_f16 v251, v138, v138, v251\n" \
    "v_mfma_f32_16x16x32_f16 v[156:159], v[48:51], v[244:247], 0\n" \
    "ds_read_b128 v[244:247], v39 offset:47840\n" \
    "v_cvt_pk_f16_f32 v139, v226, v227\n" \
    "v_mfma_f32_16x16x32_bf16 v[212:215], v[28:31], v[232:235], v[212:215]\n" \
    "ds_read_b128 v[232:235], v39 offset:44720\n" \
    "v_pk_fma_f16 v251, v139, v139, v251\n" \
    "v_mfma_f32_16x16x32_bf16 v[204:207], v[8:11], v[236:239], v[204:207]\n" \
    "v_rsq_f32 v153, v152\n" \
    "v_mfma_f32_16x16x32_f16 v[160:163], v[48:51], v[248:251], 0\n" \
    "ds_read_b128 v[248:251], v39 offset:48880\n" \
    "v_mul_f32 v153, v153, v52\n" \
    "v_mfma_f32_16x16x32_bf16 v[208:211], v[20:23], v[236:239], v[208:211]\n" \
    "v_cvt_f16_f32 v153, v153\n" \
    "v_rsq_f32 v157, v156\n" \
    "v_mfma_f32_16x16x32_bf16 v[212:215], v[32:35], v[236:239], v[212:215]\n" \
    "ds_bpermute_b32 v164, v43, v153\n" \
    "ds_bpermute_b32 v165, v43, v153 offset:64\n" \
    "s_waitcnt lgkmcnt(5)\n" \
    "v_mfma_f32_16x16x32_bf16 v[216:219], v[0:3], v[176:179], 0\n" \
    "ds_bpermute_b32 v166, v43, v153 offset:128\n" \
    "ds_bpermute_b32 v167, v43, v153 offset:192\n" \
    "v_mfma_f32_16x16x32_bf16 v[220:223], v[12:15], v[176:179], 0\n" \
    "ds_read_b128 v[236:239], v39 offset:45760\n" \
    "v_mul_f32 v157, v157, v52\n" \
    "v_mfma_f32_16x16x32_bf16 v[224:227], v[24:27], v[176:179], 0\n" \
    "v_cvt_f16_f32 v157, v157\n" \
    "v_rsq_f32 v161, v160\n" \
    "v_mfma_f32_16x16x32_bf16 v[216:219], v[4:7], v[180:183], v[216:219]\n" \
    "ds_bpermute_b32 v168, v43, v157\n" \
    "ds_bpermute_b32 v169, v43, v157 offset:64\n" \
    "v_mfma_f32_16x16x32_bf16 v[220:223], v[16:19], v[180:183], v[220:223]\n" \
    "ds_bpermute_b32 v170, v43, v157 offset:128\n" \
    "ds_bpermute_b32 v171, v43, v157 offset:192\n" \
    "v_mfma_f32_16x16x32_bf16 v[224:227], v[28:31], v[180:183], v[224:227]\n" \
    "v_mul_f32 v161, v161, v52\n" \
    "v_cvt_f16_f32 v161, v161\n" \
    "v_mfma_f32_16x16x32_bf16 v[216:219], v[8:11], v[184:187], v[216:219]\n" \
    "ds_bpermute_b32 v172, v43, v161\n" \
    "s_waitcnt lgkmcnt(8)\n" \
    "v_pk_mul_f16 v92, v92, v164 op_sel_hi:[1,0]\n" \
    "v_mfma_f32_16x16x32_bf16 v[220:223], v[20:23], v[184:187], v[220:223]\n" \
    "ds_bpermute_b32 v173, v43, v161 offset:64\n" \
    "ds_bpermute_b32 v174, v43, v161 offset:128\n" \
    "v_mfma_f32_16x16x32_bf16 v[224:227], v[32:35], v[184:187], v[224:227]\n" \
    "ds_bpermute_b32 v175, v43, v161 offset:192\n" \
    "v_pk_mul_f16 v93, v93, v164 op_sel_hi:[1,0]\n" \
    "v_pk_mul_f16 v94, v94, v164 op_sel_hi:[1,0]\n" \
    "v_pk_mul_f16 v97, v97, v165 op_sel_hi:[1,0]\n" \
    "v_pk_mul_f16 v98, v98, v165 op_sel_hi:[1,0]\n" \
    "v_pk_mul_f16 v99, v99, v165 op_sel_hi:[1,0]\n" \
    "s_waitcnt lgkmcnt(8)\n" \
    "v_pk_mul_f16 v100, v100, v166 op_sel_hi:[1,0]\n" \
    "v_pk_mul_f16 v101, v101, v166 op_sel_hi:[1,0]\n" \
    "v_pk_mul_f16 v102, v102, v166 op_sel_hi:[1,0]\n" \
    "v_pk_mul_f16 v105, v105, v167 op_sel_hi:[1,0]\n" \
    "v_pk_mul_f16 v106, v106, v167 op_sel_hi:[1,0]\n" \
    "v_pk_mul_f16 v107, v107, v167 op_sel_hi:[1,0]\n" \
    "v_min3_u32 v142, v140, v144, v148\n" \
    "s_waitcnt lgkmcnt(4)\n" \
    "v_pk_mul_f16 v108, v108, v168 op_sel_hi:[1,0]\n" \
    "v_max3_u32 v143, v140, v144, v148\n" \
    "v_pk_mul_f16 v109, v109, v168 op_sel_hi:[1,0]\n" \
    "v_min3_u32 v142, v142, v152, v156\n" \
    "v_pk_mul_f16 v110, v110, v168 op_sel_hi:[1,0]\n" \
    "v_max3_u32 v143, v143, v152, v156\n" \
    "v_pk_mul_f16 v113, v113, v169 op_sel_hi:[1,0]\n" \
    "v_min3_u32 v142, v142, v160, s67\n" \
    "v_pk_mul_f16 v114, v114, v169 op_sel_hi:[1,0]\n" \
    "v_max3_u32 v143, v143, v160, s68\n" \
    "v_pk_mul_f16 v115, v115, v169 op_sel_hi:[1,0]\n" \
    "v_sub_u32 v142, v143, v142\n" \
    "v_pk_mul_f16 v116, v116, v170 op_sel_hi:[1,0]\n" \
    "v_cmp_ne_u32 vcc, 0xb800000, v142\n" \
    "v_pk_mul_f16 v117, v117, v170 op_sel_hi:[1,0]\n" \
    "s_cbranch_vccnz Lexit%=\n" \
    "v_pk_mul_f16 v118, v118, v170 op_sel_hi:[1,0]\n" \
    "v_pk_mul_f16 v121, v121, v171 op_sel_hi:[1,0]\n" \
    "v_pk_mul_f16 v122, v122, v171 op_sel_hi:[1,0]\n" \
    "v_cvt_pk_bf16_f32 v140, v204, v205\n" \
    "v_pk_mul_f16 v123, v123, v171 op_sel_hi:[1,0]\n" \
    "v_cvt_pk_bf16_f32 v141, v206, v207\n" \
    "v_mfma_f32_16x16x32_bf16 v[204:207], v[0:3], v[228:231], 0\n" \
    "s_waitcnt lgkmcnt(0)\n" \
    "v_pk_mul_f16 v124, v124, v172 op_sel_hi:[1,0]\n" \
    "v_mfma_f32_16x16x32_bf16 v[204:207], v[4:7], v[232:235], v[204:207]\n" \
    "v_cvt_pk_bf16_f32 v142, v208, v209\n" \
    "v_pk_mul_f16 v125, v125, v172 op_sel_hi:[1,0]\n" \
    "v_mfma_f32_16x16x32_bf16 v[204:207], v[8:11], v[236:239], v[204:207]\n" \
    "v_cvt_pk_bf16_f32 v143, v210, v211\n" \
    "v_pk_mul_f16 v126, v126, v172 op_sel_hi:[1,0]\n" \
    "v_mfma_f32_16x16x32_bf16 v[208:211], v[12:15], v[228:231], 0\n" \
    "v_cvt_pk_bf16_f32 v144, v212, v213\n" \
    "v_pk_mul_f16 v129, v129, v173 op_sel_hi:[1,0]\n" \
    "v_mfma_f32_16x16x32_bf16 v[208:211], v[16:19], v[232:235], v[208:211]\n" \
    "v_cvt_pk_bf16_f32 v145, v214, v215\n" \
    "v_pk_mul_f16 v130, v130, v173 op_sel_hi:[1,0]\n" \
    "v_mfma_f32_16x16x32_bf16 v[212:215], v[24:27], v[228:231], 0\n" \
    "ds_read_b128 v[228:231], v39 offset:49920\n" \
    "v_pk_mul_f16 v131, v131, v173 op_sel_hi:[1,0]\n" \
    "v_mfma_f32_16x16x32_bf16 v[212:215], v[28:31], v[232:235], v[212:215]\n" \
    "ds_read_b128 v[232:235], v39 offset:50960\n" \
    "v_pk_mul_f16 v132, v132, v174 op_sel_hi:[1,0]\n" \
    "v_mfma_f32_16x16x32_bf16 v[208:211], v[20:23], v[236:239], v[208:211]\n" \
    "v_pk_mul_f16 v133, v133, v174 op_sel_hi:[1,0]\n" \
    "v_pk_mul_f16 v134, v134, v174 op_sel_hi:[1,0]\n" \
    "v_mfma_f32_16x16x32_bf16 v[212:215], v[32:35], v[236:239], v[212:215]\n" \
    "ds_read_b128 v[236:239], v39 offset:52000\n" \
    "v_pk_mul_f16 v137, v137, v175 op_sel_hi:[1,0]\n" \
    "v_pk_mul_f16 v138, v138, v175 op_sel_hi:[1,0]\n" \
    "v_pk_mul_f16 v139, v139, v175 op_sel_hi:[1,0]\n" \
    "v_cvt_pk_bf16_f32 v216, v216, v217\n" \
    "v_cndmask_b32 v148, v55, v216, s[58:59]\n" \
    "v_cndmask_b32 v164, v216, v55, s[58:59]\n" \
    "v_cvt_pk_bf16_f32 v218, v218, v219\n" \
    "v_cndmask_b32 v149, v55, v218, s[58:59]\n" \
    "v_cndmask_b32 v165, v218, v55, s[58:59]\n" \
    "v_mfma_f32_16x16x32_bf16 v[216:219], v[0:3], v[240:243], 0\n" \
    "v_cvt_pk_bf16_f32 v220, v220, v221\n" \
    "v_cndmask_b32 v150, v55, v220, s[58:59]\n" \
    "v_mfma_f32_16x16x32_bf16 v[216:219], v[4:7], v[244:247], v[216:219]\n" \
    "v_cndmask_b32 v166, v220, v55, s[58:59]\n" \
    "v_cvt_pk_bf16_f32 v222, v222, v223\n" \
    "v_mfma_f32_16x16x32_bf16 v[216:219], v[8:11], v[248:251], v[216:219]\n" \
    "v_cndmask_b32 v151, v55, v222, s[58:59]\n" \
    "v_cndmask_b32 v167, v222, v55, s[58:59]\n" \
    "v_mfma_f32_16x16x32_bf16 v[220:223], v[12:15], v[240:243], 0\n" \
    "v_cvt_pk_bf16_f32 v224, v224, v225\n" \
    "v_cndmask_b32 v152, v55, v224, s[58:59]\n" \
    "v_mfma_f32_16x16x32_bf16 v[220:223], v[16:19], v[244:247], v[220:223]\n" \
    "v_cndmask_b32 v168, v224, v55, s[58:59]\n" \
    "v_cvt_pk_bf16_f32 v226, v226, v227\n" \
    "v_mfma_f32_16x16x32_bf16 v[220:223], v[20:23], v[248:251], v[220:223]\n" \
    "v_cndmask_b32 v153, v55, v226, s[58:59]\n" \
    "v_cndmask_b32 v169, v226, v55, s[58:59]\n" \
    "v_mfma_f32_16x16x32_bf16 v[224:227], v[24:27], v[240:243], 0\n" \
    "ds_read_b128 v[240:243], v39 offset:53040\n" \
    "v_cvt_pk_bf16_f32 v156, v204, v205\n" \
    "v_mfma_f32_16x16x32_bf16 v[224:227], v[28:31], v[244:247], v[224:227]\n" \
    "ds_read_b128 v[244:247], v39 offset:54080\n" \
    "v_cvt_pk_bf16_f32 v157, v206, v207\n" \
    "v_mfma_f32_16x16x32_bf16 v[224:227], v[32:35], v[248:251], v[224:227]\n" \
    "ds_read_b128 v[248:251], v39 offset:55120\n" \
    "v_cvt_pk_bf16_f32 v158, v208, v209\n" \
    "s_waitcnt lgkmcnt(3)\n" \
    "v_mfma_f32_16x16x32_bf16 v[204:207], v[0:3], v[228:231], 0\n" \
    "v_cvt_pk_bf16_f32 v159, v210, v211\n" \
    "v_cvt_pk_bf16_f32 v160, v212, v213\n" \
    "v_mfma_f32_16x16x32_bf16 v[208:211], v[12:15], v[228:231], 0\n" \
    "v_cvt_pk_bf16_f32 v161, v214, v215\n" \
    "v_cvt_pk_bf16_f32 v172, v216, v217\n" \
    "v_mfma_f32_16x16x32_bf16 v[212:215], v[24:27], v[228:231], 0\n" \
    "v_cvt_pk_bf16_f32 v173, v218, v219\n" \
    "v_cvt_pk_bf16_f32 v174, v220, v221\n" \
    "v_mfma_f32_16x16x32_bf16 v[204:207], v[4:7], v[232:235], v[204:207]\n" \
    "v_cvt_pk_bf16_f32 v175, v222, v223\n" \
    "v_cvt_pk_bf16_f32 v176, v224, v225\n" \
    "v_mfma_f32_16x16x32_bf16 v[208:211], v[16:19], v[232:235], v[208:211]\n" \
    "v_cvt_pk_bf16_f32 v177, v226, v227\n" \
    "v_mfma_f32_16x16x32_bf16 v[212:215], v[28:31], v[232:235], v[212:215]\n" \
    "v_mfma_f32_16x16x32_bf16 v[204:207], v[8:11], v[236:239], v[204:207]\n" \
    "v_mfma_f32_16x16x32_bf16 v[208:211], v[20:23], v[236:239], v[208:211]\n" \
    "v_mfma_f32_16x16x32_bf16 v[212:215], v[32:35], v[236:239], v[212:215]\n" \
    "s_waitcnt lgkmcnt(0)\n" \
    "v_mfma_f32_16x16x32_bf16 v[216:219], v[0:3], v[240:243], 0\n" \
    "v_mfma_f32_16x16x32_bf16 v[220:223], v[12:15], v[240:243], 0\n" \
    "v_mfma_f32_16x16x32_bf16 v[224:227], v[24:27], v[240:243], 0\n" \
    "s_nop 1\n" \
    "v_cvt_pk_bf16_f32 v204, v204, v205\n" \
    "v_mfma_f32_16x16x32_bf16 v[216:219], v[4:7], v[244:247], v[216:219]\n" \
    "v_cndmask_b32 v180, v55, v204, s[58:59]\n" \
    "v_cndmask_b32 v196, v204, v55, s[58:59]\n" \
    "v_mfma_f32_16x16x32_bf16 v[220:223], v[16:19], v[244:247], v[220:223]\n" \
    "v_cvt_pk_bf16_f32 v206, v206, v207\n" \
    "v_cndmask_b32 v181, v55, v206, s[58:59]\n" \
    "v_mfma_f32_16x16x32_bf16 v[224:227], v[28:31], v[244:247], v[224:227]\n" \
    "v_cndmask_b32 v197, v206, v55, s[58:59]\n" \
    "v_cvt_pk_bf16_f32 v208, v208, v209\n" \
    "v_mfma_f32_16x16x32_bf16 v[216:219], v[8:11], v[248:251], v[216:219]\n" \
    "v_cndmask_b32 v182, v55, v208, s[58:59]\n" \
    "v_cndmask_b32 v198, v208, v55, s[58:59]\n" \
    "v_mfma_f32_16x16x32_bf16 v[220:223], v[20:23], v[248:251], v[220:223]\n" \
    "v_cvt_pk_bf16_f32 v210, v210, v211\n" \
    "v_cndmask_b32 v183, v55, v210, s[58:59]\n" \
    "v_mfma_f32_16x16x32_bf16 v[224:227], v[32:35], v[248:251], v[224:227]\n" \
    "v_cndmask_b32 v199, v210, v55, s[58:59]\n" \
    "v_cvt_pk_bf16_f32 v212, v212, v213\n" \
    "v_cndmask_b32 v184, v55, v212, s[58:59]\n" \
    "v_cndmask_b32 v200, v212, v55, s[58:59]\n" \
    "v_cvt_pk_bf16_f32 v214, v214, v215\n" \
    "v_cndmask_b32 v185, v55, v214, s[58:59]\n" \
    "v_cndmask_b32 v201, v214, v55, s[58:59]\n" \
    "v_mov_b32 v55, 0x30000\n" \
    "v_cndmask_b32 v146, v55, v54, s[76:77]\n" \
    "v_cvt_pk_bf16_f32 v188, v216, v217\n" \
    "v_cvt_pk_bf16_f32 v189, v218, v219\n" \
    "v_cndmask_b32 v162, v55, v54, s[78:79]\n" \
    "v_cvt_pk_bf16_f32 v190, v220, v221\n" \
    "v_cvt_pk_bf16_f32 v191, v222, v223\n" \
    "v_cvt_pk_bf16_f32 v192, v224, v225\n" \
    "v_cndmask_b32 v178, v55, v54, s[80:81]\n" \
    "v_cvt_pk_bf16_f32 v193, v226, v227\n" \
    "s_branch Lmt%=\n" \
    "Lr1m%=:\n" \
    "ds_read_b128 v[236:239], v44\n" \
    "ds_read_b128 v[224:227], v44 offset:256\n" \
    "ds_read_b128 v[240:243], v45\n" \
    "v_cndmask_b32 v194, v55, v54, s[82:83]\n" \
    "ds_read_b64 v[154:155], v146 offset:1152\n" \
    "ds_read_b128 v[228:231], v45 offset:256\n" \
    "ds_read_b64 v[146:147], v146 offset:1024\n" \
    "ds_read_b128 v[244:247], v46\n" \
    "ds_read_b128 v[232:235], v46 offset:256\n" \
    "ds_read_b32 v24, v53 offset:12288\n" \
    "ds_read_b64 v[170:171], v162 offset:1408\n" \
    "ds_read_b128 v[248:251], v47\n" \
    "ds_read_b64 v[162:163], v162 offset:1280\n" \
    "ds_read_b128 v[252:255], v47 offset:256\n" \
    "ds_read_b32 v28, v53 offset:12292\n" \
    "ds_read_b128 v[204:207], v41 offset:36992\n" \
    "s_waitcnt lgkmcnt(0)\n" \
    "v_add_f32 v204, v204, v24\n" \
    "ds_read_b64 v[186:187], v178 offset:1664\n" \
    "ds_read_b128 v[208:211], v41 offset:38016\n" \
    "v_mfma_f32_16x16x32_f16 v[204:207], v[236:239], v[92:95], v[204:207]\n" \
    "ds_read_b32 v32, v53 offset:12296\n" \
    "ds_read_b64 v[178:179], v178 offset:1536\n" \
    "v_mfma_f32_16x16x32_f16 v[204:207], v[240:243], v[96:99], v[204:207]\n" \
    "ds_read_b128 v[212:215], v41 offset:39040\n" \
    "ds_read_b128 v[216:219], v42 offset:192\n" \
    "v_mfma_f32_16x16x32_f16 v[204:207], v[244:247], v[100:103], v[204:207]\n" \
    "ds_read_b32 v24, v53 offset:12300\n" \
    "ds_read_b64 v[202:203], v194 offset:1920\n" \
    "v_mfma_f32_16x16x32_f16 v[204:207], v[248:251], v[104:107], v[204:207]\n" \
    "ds_read_b128 v[0:3], v42\n" \
    "ds_read_b64 v[194:195], v194 offset:1792\n" \
    "ds_read_b128 v[4:7], v42 offset:64\n" \
    "ds_read_b128 v[8:11], v42 offset:128\n" \
    "ds_read_b128 v[12:15], v41\n" \
    "ds_read_b128 v[16:19], v41 offset:1024\n" \
    "ds_read_b128 v[20:23], v41 offset:2048\n" \
    "s_nop 0\n" \
    "v_exp_f32 v204, v204\n" \
    "s_nop 0\n" \
    "v_cvt_pk_bf16_f32 v206, v204, 0\n" \
    "s_waitcnt lgkmcnt(13)\n" \
    "v_add_f32 v208, v208, v28\n" \
    "ds_read_b128 v[28:31], v53 offset:9216\n" \
    "ds_read_b32 v207, v55\n" \
    "v_mfma_f32_16x16x32_f16 v[208:211], v[236:239], v[108:111], v[208:211]\n" \
    "s_waitcnt lgkmcnt(12)\n" \
    "v_add_f32 v212, v212, v32\n" \
    "v_mfma_f32_16x16x32_f16 v[208:211], v[240:243], v[112:115], v[208:211]\n" \
    "ds_read_b128 v[32:35], v53 offset:10240\n" \
    "s_waitcnt lgkmcnt(10)\n" \
    "v_add_f32 v216, v216, v24\n" \
    "v_mfma_f32_16x16x32_f16 v[212:215], v[236:239], v[124:127], v[212:215]\n" \
    "ds_read_b128 v[24:27], v53 offset:11264\n" \
    "v_mfma_f32_16x16x32_f16 v[216:219], v[236:239], v[224:227], v[216:219]\n" \
    "v_mfma_f32_16x16x32_f16 v[212:215], v[240:243], v[128:131], v[212:215]\n" \
    "v_mfma_f32_16x16x32_f16 v[216:219], v[240:243], v[228:231], v[216:219]\n" \
    "v_mfma_f32_16x16x32_f16 v[208:211], v[244:247], v[116:119], v[208:211]\n" \
    "s_waitcnt lgkmcnt(2)\n" \
    "v_add_f32 v0, v0, v28\n" \
    "v_mfma_f32_16x16x32_f16 v[212:215], v[244:247], v[132:135], v[212:215]\n" \
    "v_add_f32 v1, v1, v29\n" \
    "v_add_f32 v2, v2, v30\n" \
    "v_mfma_f32_16x16x32_f16 v[216:219], v[244:247], v[232:235], v[216:219]\n" \
    "v_add_f32 v3, v3, v31\n" \
    "s_waitcnt lgkmcnt(1)\n" \
    "v_add_f32 v4, v4, v32\n" \
    "v_mfma_f32_16x16x32_f16 v[0:3], v[56:59], v[224:227], v[0:3]\n" \
    "ds_read_b128 v[28:31], v53 offset:0\n" \
    "v_add_f32 v5, v5, v33\n" \
    "v_mfma_f32_16x16x32_f16 v[0:3], v[58:61], v[228:231], v[0:3]\n" \
    "v_add_f32 v6, v6, v34\n" \
    "v_add_f32 v7, v7, v35\n" \
    "v_mfma_f32_16x16x32_f16 v[0:3], v[62:65], v[232:235], v[0:3]\n" \
    "ds_read_b128 v[32:35], v53 offset:1024\n" \
    "s_waitcnt lgkmcnt(2)\n" \
    "v_add_f32 v8, v8, v24\n" \
    "v_mfma_f32_16x16x32_f16 v[4:7], v[68:71], v[224:227], v[4:7]\n" \
    "v_add_f32 v9, v9, v25\n" \
    "v_add_f32 v10, v10, v26\n" \
    "v_mfma_f32_16x16x32_f16 v[4:7], v[70:73], v[228:231], v[4:7]\n" \
    "v_add_f32 v11, v11, v27\n" \
    "ds_read_b128 v[24:27], v53 offset:2048\n" \
    "v_mfma_f32_16x16x32_f16 v[4:7], v[74:77], v[232:235], v[4:7]\n" \
    "v_mfma_f32_16x16x32_f16 v[8:11], v[80:83], v[224:227], v[8:11]\n" \
    "s_waitcnt lgkmcnt(2)\n" \
    "v_add_f32 v12, v12, v28\n" \
    "v_mfma_f32_16x16x32_f16 v[8:11], v[82:85], v[228:231], v[8:11]\n" \
    "v_add_f32 v13, v13, v29\n" \
    "v_add_f32 v14, v14, v30\n" \
    "v_mfma_f32_16x16x32_f16 v[8:11], v[86:89], v[232:235], v[8:11]\n" \
    "v_add_f32 v15, v15, v31\n" \
    "s_waitcnt lgkmcnt(1)\n" \
    "v_add_f32 v16, v16, v32\n" \
    "v_mfma_f32_16x16x32_f16 v[208:211], v[248:251], v[120:123], v[208:211]\n" \
    "ds_read_b128 v[28:31], v53 offset:3072\n" \
    "v_add_f32 v17, v17, v33\n" \
    "v_mfma_f32_16x16x32_f16 v[212:215], v[248:251], v[136:139], v[212:215]\n" \
    "v_add_f32 v18, v18, v34\n" \
    "v_add_f32 v19, v19, v35\n" \
    "v_mfma_f32_16x16x32_f16 v[216:219], v[248:251], v[252:255], v[216:219]\n" \
    "ds_read_b128 v[32:35], v53 offset:4096\n" \
    "s_waitcnt lgkmcnt(2)\n" \
    "v_add_f32 v20, v20, v24\n" \
    "v_mfma_f32_16x16x32_f16 v[0:3], v[64:67], v[252:255], v[0:3]\n" \
    "ds_read_b32 v211, v55\n" \
    "ds_read_b32 v215, v55\n" \
    "v_mfma_f32_16x16x32_f16 v[4:7], v[76:79], v[252:255], v[4:7]\n" \
    "v_exp_f32 v208, v208\n" \
    "v_mfma_f32_16x16x32_f16 v[8:11], v[88:91], v[252:255], v[8:11]\n" \
    "ds_read_b32 v219, v55\n" \
    "v_cvt_pk_bf16_f32 v210, v208, 0\n" \
    "v_mfma_f32_16x16x32_f16 v[12:15], v[56:59], v[92:95], v[12:15]\n" \
    "v_exp_f32 v212, v212\n" \
    "v_mfma_f32_16x16x32_f16 v[16:19], v[68:71], v[92:95], v[16:19]\n" \
    "v_cvt_pk_bf16_f32 v214, v212, 0\n" \
    "v_exp_f32 v216, v216\n" \
    "v_exp_f32 v0, v0\n" \
    "v_cvt_pk_bf16_f32 v218, v216, 0\n" \
    "v_exp_f32 v1, v1\n" \
    "v_exp_f32 v2, v2\n" \
    "v_cvt_pk_bf16_f32 v220, v0, v1\n" \
    "v_exp_f32 v3, v3\n" \
    "v_exp_f32 v4, v4\n" \
    "v_cvt_pk_bf16_f32 v221, v2, v3\n" \
    "v_exp_f32 v5, v5\n" \
    "v_exp_f32 v6, v6\n" \
    "v_cvt_pk_bf16_f32 v222, v4, v5\n" \
    "v_exp_f32 v7, v7\n" \
    "v_exp_f32 v8, v8\n" \
    "v_cvt_pk_bf16_f32 v223, v6, v7\n" \
    "v_exp_f32 v9, v9\n" \
    "v_exp_f32 v10, v10\n" \
    "v_mfma_f32_16x16x32_bf16 v[248:251], v[148:151], v[220:223], 0\n" \
    "ds_read_b128 v[0:3], v41 offset:3072\n" \
    "ds_read_b128 v[4:7], v41 offset:4096\n" \
    "v_mfma_f32_16x16x32_bf16 v[244:247], v[140:143], v[220:223], 0\n" \
    "v_cvt_pk_bf16_f32 v216, v8, v9\n" \
    "v_exp_f32 v11, v11\n" \
    "v_add_f32 v21, v21, v25\n" \
    "v_cvt_pk_bf16_f32 v217, v10, v11\n" \
    "v_add_f32 v22, v22, v26\n" \
    "v_add_f32 v23, v23, v27\n" \
    "s_waitcnt lgkmcnt(2)\n" \
    "v_mfma_f32_16x16x32_bf16 v[248:251], v[152:155], v[216:219], v[248:251]\n" \
    "ds_read_b128 v[8:11], v41 offset:5120\n" \
    "ds_read_b128 v[24:27], v53 offset:5120\n" \
    "v_mfma_f32_16x16x32_f16 v[20:23], v[80:83], v[92:95], v[20:23]\n" \
    "v_exp_f32 v12, v12\n" \
    "v_mfma_f32_16x16x32_bf16 v[244:247], v[144:147], v[216:219], v[244:247]\n" \
    "v_exp_f32 v13, v13\n" \
    "v_exp_f32 v14, v14\n" \
    "v_cvt_pk_bf16_f32 v12, v12, v13\n" \
    "v_exp_f32 v15, v15\n" \
    "s_waitcnt lgkmcnt(2)\n" \
    "v_add_f32 v0, v0, v28\n" \
    "v_add_f32 v1, v1, v29\n" \
    "v_add_f32 v2, v2, v30\n" \
    "v_add_f32 v3, v3, v31\n" \
    "v_add_f32 v4, v4, v32\n" \
    "v_add_f32 v5, v5, v33\n" \
    "v_mfma_f32_16x16x32_f16 v[0:3], v[56:59], v[108:111], v[0:3]\n" \
    "ds_read_b128 v[28:31], v53 offset:6144\n" \
    "ds_write_b128 v44, v[244:247] offset:512\n" \
    "ds_write_b128 v44, v[248:251] offset:768\n" \
    "v_add_f32 v6, v6, v34\n" \
    "v_add_f32 v7, v7, v35\n" \
    "s_waitcnt lgkmcnt(3)\n" \
    "v_add_f32 v8, v8, v24\n" \
    "v_mfma_f32_16x16x32_f16 v[4:7], v[68:71], v[108:111], v[4:7]\n" \
    "ds_read_b128 v[32:35], v53 offset:7168\n" \
    "v_add_f32 v9, v9, v25\n" \
    "v_add_f32 v10, v10, v26\n" \
    "v_add_f32 v11, v11, v27\n" \
    "v_cvt_pk_bf16_f32 v13, v14, v15\n" \
    "v_exp_f32 v16, v16\n" \
    "v_mfma_f32_16x16x32_f16 v[8:11], v[80:83], v[108:111], v[8:11]\n" \
    "ds_read_b128 v[24:27], v53 offset:8192\n" \
    "v_exp_f32 v17, v17\n" \
    "v_exp_f32 v18, v18\n" \
    "v_cvt_pk_bf16_f32 v14, v16, v17\n" \
    "v_exp_f32 v19, v19\n" \
    "v_exp_f32 v20, v20\n" \
    "v_cvt_pk_bf16_f32 v15, v18, v19\n" \
    "v_exp_f32 v21, v21\n" \
    "v_exp_f32 v22, v22\n" \
    "v_mfma_f32_16x16x32_bf16 v[240:243], v[148:151], v[12:15], 0\n" \
    "ds_read_b128 v[16:19], v41 offset:7168\n" \
    "v_cvt_pk_bf16_f32 v204, v20, v21\n" \
    "v_mfma_f32_16x16x32_bf16 v[236:239], v[140:143], v[12:15], 0\n" \
    "ds_read_b128 v[12:15], v41 offset:6144\n" \
    "v_exp_f32 v23, v23\n" \
    "v_exp_f32 v0, v0\n" \
    "v_cvt_pk_bf16_f32 v205, v22, v23\n" \
    "v_exp_f32 v1, v1\n" \
    "v_exp_f32 v2, v2\n" \
    "v_mfma_f32_16x16x32_bf16 v[240:243], v[152:155], v[204:207], v[240:243]\n" \
    "ds_read_b128 v[20:23], v41 offset:8192\n" \
    "v_cvt_pk_bf16_f32 v0, v0, v1\n" \
    "v_mfma_f32_16x16x32_bf16 v[236:239], v[144:147], v[204:207], v[236:239]\n" \
    "v_exp_f32 v3, v3\n" \
    "v_exp_f32 v4, v4\n" \
    "v_cvt_pk_bf16_f32 v1, v2, v3\n" \
    "v_exp_f32 v5, v5\n" \
    "v_exp_f32 v6, v6\n" \
    "v_min_u32 v252, s60, v243\n" \
    "v_max_u32 v253, s62, v243\n" \
    "v_rcp_f32 v242, v242\n" \
    "s_waitcnt lgkmcnt(1)\n" \
    "v_add_f32 v12, v12, v28\n" \
    "v_mul_f32 v236, v236, v242\n" \
    "v_add_f32 v13, v13, v29\n" \
    "v_mul_f32 v237, v237, v242\n" \
    "v_add_f32 v14, v14, v30\n" \
    "v_cvt_pk_bf16_f32 v224, v236, v237\n" \
    "v_add_f32 v15, v15, v31\n" \
    "v_mul_f32 v238, v238, v242\n" \
    "v_mul_f32 v239, v239, v242\n" \
    "v_mfma_f32_16x16x32_f16 v[12:15], v[56:59], v[124:127], v[12:15]\n" \
    "ds_read_b128 v[28:31], v53 offset:0\n" \
    "v_add_f32 v16, v16, v32\n" \
    "v_cvt_pk_bf16_f32 v225, v238, v239\n" \
    "v_add_f32 v17, v17, v33\n" \
    "v_mul_f32 v240, v240, v242\n" \
    "v_add_f32 v18, v18, v34\n" \
    "v_mul_f32 v241, v241, v242\n" \
    "v_add_f32 v19, v19, v35\n" \
    "v_cvt_pk_bf16_f32 v226, v240, v241\n" \
    "s_waitcnt lgkmcnt(1)\n" \
    "v_add_f32 v20, v20, v24\n" \
    "v_mfma_f32_16x16x32_f16 v[16:19], v[68:71], v[124:127], v[16:19]\n" \
    "ds_read_b128 v[32:35], v53 offset:1024\n" \
    "v_add_f32 v21, v21, v25\n" \
    "v_add_f32 v22, v22, v26\n" \
    "v_add_f32 v23, v23, v27\n" \
    "v_min_u32 v252, v252, v251\n" \
    "v_max_u32 v253, v253, v251\n" \
    "v_mfma_f32_16x16x32_f16 v[20:23], v[80:83], v[124:127], v[20:23]\n" \
    "ds_read_b128 v[24:27], v53 offset:2048\n" \
    "v_cvt_pk_bf16_f32 v2, v4, v5\n" \
    "v_exp_f32 v7, v7\n" \
    "v_exp_f32 v8, v8\n" \
    "v_cvt_pk_bf16_f32 v3, v6, v7\n" \
    "v_exp_f32 v9, v9\n" \
    "v_exp_f32 v10, v10\n" \
    "v_mfma_f32_16x16x32_bf16 v[240:243], v[148:151], v[0:3], 0\n" \
    "ds_read_b128 v[4:7], v41 offset:10272\n" \
    "v_cvt_pk_bf16_f32 v208, v8, v9\n" \
    "v_mfma_f32_16x16x32_bf16 v[236:239], v[140:143], v[0:3], 0\n" \
    "ds_read_b128 v[0:3], v41 offset:9248\n" \
    "v_exp_f32 v11, v11\n" \
    "v_exp_f32 v12, v12\n" \
    "v_cvt_pk_bf16_f32 v209, v10, v11\n" \
    "v_exp_f32 v13, v13\n" \
    "v_exp_f32 v14, v14\n" \
    "v_mfma_f32_16x16x32_bf16 v[240:243], v[152:155], v[208:211], v[240:243]\n" \
    "ds_read_b128 v[8:11], v41 offset:11296\n" \
    "v_cvt_pk_bf16_f32 v12, v12, v13\n" \
    "v_mfma_f32_16x16x32_bf16 v[236:239], v[144:147], v[208:211], v[236:239]\n" \
    "v_exp_f32 v15, v15\n" \
    "v_exp_f32 v16, v16\n" \
    "v_cvt_pk_bf16_f32 v13, v14, v15\n" \
    "v_exp_f32 v17, v17\n" \
    "v_exp_f32 v18, v18\n" \
    "s_waitcnt lgkmcnt(2)\n" \
    "v_add_f32 v4, v4, v32\n" \
    "v_min_u32 v252, v252, v243\n" \
    "s_waitcnt lgkmcnt(1)\n" \
    "v_add_f32 v0, v0, v28\n" \
    "v_add_f32 v1, v1, v29\n" \
    "v_add_f32 v2, v2, v30\n" \
    "v_add_f32 v3, v3, v31\n" \
    "v_add_f32 v5, v5, v33\n" \
    "v_max_u32 v253, v253, v243\n" \
    "v_mfma_f32_16x16x32_f16 v[0:3], v[58:61], v[96:99], v[0:3]\n" \
    "ds_read_b128 v[28:31], v53 offset:3072\n" \
    "v_add_f32 v6, v6, v34\n" \
    "v_rcp_f32 v242, v242\n" \
    "v_add_f32 v7, v7, v35\n" \
    "v_mul_f32 v236, v236, v242\n" \
    "v_mul_f32 v237, v237, v242\n" \
    "v_mfma_f32_16x16x32_f16 v[4:7], v[70:73], v[96:99], v[4:7]\n" \
    "ds_read_b128 v[32:35], v53 offset:4096\n" \
    "s_waitcnt lgkmcnt(2)\n" \
    "v_add_f32 v8, v8, v24\n" \
    "v_cvt_pk_bf16_f32 v228, v236, v237\n" \
    "v_add_f32 v9, v9, v25\n" \
    "v_mul_f32 v238, v238, v242\n" \
    "v_add_f32 v10, v10, v26\n" \
    "v_mul_f32 v239, v239, v242\n" \
    "v_add_f32 v11, v11, v27\n" \
    "v_cvt_pk_bf16_f32 v229, v238, v239\n" \
    "v_mul_f32 v240, v240, v242\n" \
    "v_mfma_f32_16x16x32_f16 v[8:11], v[82:85], v[96:99], v[8:11]\n" \
    "ds_read_b128 v[24:27], v53 offset:5120\n" \
    "v_mul_f32 v241, v241, v242\n" \
    "v_cvt_pk_bf16_f32 v230, v240, v241\n" \
    "v_cvt_pk_bf16_f32 v14, v16, v17\n" \
    "v_exp_f32 v19, v19\n" \
    "v_exp_f32 v20, v20\n" \
    "v_cvt_pk_bf16_f32 v15, v18, v19\n" \
    "v_exp_f32 v21, v21\n" \
    "v_exp_f32 v22, v22\n" \
    "v_mfma_f32_16x16x32_bf16 v[248:251], v[148:151], v[12:15], 0\n" \
    "ds_read_b128 v[16:19], v41 offset:13344\n" \
    "v_cvt_pk_bf16_f32 v212, v20, v21\n" \
    "v_mfma_f32_16x16x32_bf16 v[244:247], v[140:143], v[12:15], 0\n" \
    "ds_read_b128 v[12:15], v41 offset:12320\n" \
    "v_exp_f32 v23, v23\n" \
    "v_exp_f32 v0, v0\n" \
    "v_cvt_pk_bf16_f32 v213, v22, v23\n" \
    "v_exp_f32 v1, v1\n" \
    "v_exp_f32 v2, v2\n" \
    "v_mfma_f32_16x16x32_bf16 v[248:251], v[152:155], v[212:215], v[248:251]\n" \
    "ds_read_b128 v[20:23], v41 offset:14368\n" \
    "v_cvt_pk_bf16_f32 v0, v0, v1\n" \
    "v_mfma_f32_16x16x32_bf16 v[244:247], v[144:147], v[212:215], v[244:247]\n" \
    "v_exp_f32 v3, v3\n" \
    "v_exp_f32 v4, v4\n" \
    "v_cvt_pk_bf16_f32 v1, v2, v3\n" \
    "v_exp_f32 v5, v5\n" \
    "v_exp_f32 v6, v6\n" \
    "s_waitcnt lgkmcnt(2)\n" \
    "v_add_f32 v16, v16, v32\n" \
    "v_min_u32 v252, v252, v251\n" \
    "s_waitcnt lgkmcnt(1)\n" \
    "v_add_f32 v12, v12, v28\n" \
    "v_add_f32 v13, v13, v29\n" \
    "v_add_f32 v14, v14, v30\n" \
    "v_add_f32 v15, v15, v31\n" \
    "v_add_f32 v17, v17, v33\n" \
    "v_max_u32 v253, v253, v251\n" \
    "v_mfma_f32_16x16x32_f16 v[12:15], v[58:61], v[112:115], v[12:15]\n" \
    "ds_read_b128 v[28:31], v53 offset:6144\n" \
    "v_add_f32 v18, v18, v34\n" \
    "v_rcp_f32 v250, v250\n" \
    "v_add_f32 v19, v19, v35\n" \
    "v_mul_f32 v244, v244, v250\n" \
    "v_mul_f32 v245, v245, v250\n" \
    "v_mfma_f32_16x16x32_f16 v[16:19], v[70:73], v[112:115], v[16:19]\n" \
    "ds_read_b128 v[32:35], v53 offset:7168\n" \
    "s_waitcnt lgkmcnt(2)\n" \
    "v_add_f32 v20, v20, v24\n" \
    "v_cvt_pk_bf16_f32 v232, v244, v245\n" \
    "v_add_f32 v21, v21, v25\n" \
    "v_mul_f32 v246, v246, v250\n" \
    "v_add_f32 v22, v22, v26\n" \
    "v_mul_f32 v247, v247, v250\n" \
    "v_add_f32 v23, v23, v27\n" \
    "v_cvt_pk_bf16_f32 v233, v246, v247\n" \
    "v_mfma_f32_16x16x32_bf16 v[244:247], v[156:159], v[220:223], 0\n" \
    "ds_read_b128 v[24:27], v53 offset:8192\n" \
    "v_mul_f32 v248, v248, v250\n" \
    "v_mfma_f32_16x16x32_f16 v[20:23], v[82:85], v[112:115], v[20:23]\n" \
    "v_mul_f32 v249, v249, v250\n" \
    "v_cvt_pk_bf16_f32 v234, v248, v249\n" \
    "v_mfma_f32_16x16x32_bf16 v[248:251], v[164:167], v[220:223], 0\n" \
    "v_cvt_pk_bf16_f32 v2, v4, v5\n" \
    "v_exp_f32 v7, v7\n" \
    "v_mfma_f32_16x16x32_bf16 v[248:251], v[168:171], v[216:219], v[248:251]\n" \
    "v_cvt_pk_bf16_f32 v3, v6, v7\n" \
    "v_exp_f32 v8, v8\n" \
    "v_mfma_f32_16x16x32_bf16 v[244:247], v[160:163], v[216:219], v[244:247]\n" \
    "ds_read_b128 v[4:7], v41 offset:16416\n" \
    "v_exp_f32 v9, v9\n" \
    "v_mfma_f32_16x16x32_bf16 v[240:243], v[164:167], v[0:3], 0\n" \
    "v_cvt_pk_bf16_f32 v204, v8, v9\n" \
    "v_exp_f32 v10, v10\n" \
    "v_mfma_f32_16x16x32_bf16 v[236:239], v[156:159], v[0:3], 0\n" \
    "ds_read_b128 v[0:3], v41 offset:15392\n" \
    "v_exp_f32 v11, v11\n" \
    "v_exp_f32 v12, v12\n" \
    "v_cvt_pk_bf16_f32 v205, v10, v11\n" \
    "v_exp_f32 v13, v13\n" \
    "v_exp_f32 v14, v14\n" \
    "v_mfma_f32_16x16x32_bf16 v[240:243], v[168:171], v[204:207], v[240:243]\n" \
    "ds_read_b128 v[8:11], v41 offset:17440\n" \
    "ds_write_b128 v45, v[244:247] offset:512\n" \
    "v_mfma_f32_16x16x32_bf16 v[236:239], v[160:163], v[204:207], v[236:239]\n" \
    "ds_write_b128 v45, v[248:251] offset:768\n" \
    "s_waitcnt lgkmcnt(3)\n" \
    "v_add_f32 v0, v0, v28\n" \
    "v_add_f32 v1, v1, v29\n" \
    "v_add_f32 v2, v2, v30\n" \
    "v_min_u32 v252, v252, v241\n" \
    "v_max_u32 v253, v253, v241\n" \
    "v_rcp_f32 v240, v240\n" \
    "v_add_f32 v3, v3, v31\n" \
    "v_mul_f32 v242, v242, v240\n" \
    "v_mul_f32 v243, v243, v240\n" \
    "v_mfma_f32_16x16x32_f16 v[0:3], v[58:61], v[128:131], v[0:3]\n" \
    "ds_read_b128 v[28:31], v53 offset:0\n" \
    "v_cvt_pk_bf16_f32 v227, v242, v243\n" \
    "v_mul_f32 v236, v236, v240\n" \
    "v_mul_f32 v237, v237, v240\n" \
    "v_add_f32 v4, v4, v32\n" \
    "v_cvt_pk_bf16_f32 v140, v236, v237\n" \
    "v_add_f32 v5, v5, v33\n" \
    "v_mul_f32 v238, v238, v240\n" \
    "v_add_f32 v6, v6, v34\n" \
    "v_mul_f32 v239, v239, v240\n" \
    "v_add_f32 v7, v7, v35\n" \
    "v_cvt_pk_bf16_f32 v141, v238, v239\n" \
    "s_waitcnt lgkmcnt(1)\n" \
    "v_add_f32 v8, v8, v24\n" \
    "v_mfma_f32_16x16x32_f16 v[4:7], v[70:73], v[128:131], v[4:7]\n" \
    "ds_read_b128 v[32:35], v53 offset:1024\n" \
    "v_add_f32 v9, v9, v25\n" \
    "v_add_f32 v10, v10, v26\n" \
    "v_add_f32 v11, v11, v27\n" \
    "v_min_u32 v252, v252, v249\n" \
    "v_max_u32 v253, v253, v249\n" \
    "v_mfma_f32_16x16x32_f16 v[8:11], v[82:85], v[128:131], v[8:11]\n" \
    "ds_read_b128 v[24:27], v53 offset:2048\n" \
    "v_cvt_pk_bf16_f32 v12, v12, v13\n" \
    "v_exp_f32 v15, v15\n" \
    "v_exp_f32 v16, v16\n" \
    "v_cvt_pk_bf16_f32 v13, v14, v15\n" \
    "v_exp_f32 v17, v17\n" \
    "v_exp_f32 v18, v18\n" \
    "v_cvt_pk_bf16_f32 v14, v16, v17\n" \
    "v_exp_f32 v19, v19\n" \
    "v_exp_f32 v20, v20\n" \
    "v_cvt_pk_bf16_f32 v15, v18, v19\n" \
    "v_exp_f32 v21, v21\n" \
    "v_exp_f32 v22, v22\n" \
    "v_mfma_f32_16x16x32_bf16 v[240:243], v[164:167], v[12:15], 0\n" \
    "ds_read_b128 v[16:19], v41 offset:19520\n" \
    "v_cvt_pk_bf16_f32 v208, v20, v21\n" \
    "v_mfma_f32_16x16x32_bf16 v[236:239], v[156:159], v[12:15], 0\n" \
    "ds_read_b128 v[12:15], v41 offset:18496\n" \
    "v_exp_f32 v23, v23\n" \
    "v_exp_f32 v0, v0\n" \
    "v_cvt_pk_bf16_f32 v209, v22, v23\n" \
    "v_exp_f32 v1, v1\n" \
    "v_exp_f32 v2, v2\n" \
    "v_mfma_f32_16x16x32_bf16 v[240:243], v[168:171], v[208:211], v[240:243]\n" \
    "ds_read_b128 v[20:23], v41 offset:20544\n" \
    "v_cvt_pk_bf16_f32 v0, v0, v1\n" \
    "v_mfma_f32_16x16x32_bf16 v[236:239], v[160:163], v[208:211], v[236:239]\n" \
    "v_exp_f32 v3, v3\n" \
    "v_exp_f32 v4, v4\n" \
    "v_cvt_pk_bf16_f32 v1, v2, v3\n" \
    "v_exp_f32 v5, v5\n" \
    "v_exp_f32 v6, v6\n" \
    "s_waitcnt lgkmcnt(2)\n" \
    "v_add_f32 v16, v16, v32\n" \
    "v_min_u32 v252, v252, v241\n" \
    "s_waitcnt lgkmcnt(1)\n" \
    "v_add_f32 v12, v12, v28\n" \
    "v_add_f32 v13, v13, v29\n" \
    "v_add_f32 v14, v14, v30\n" \
    "v_add_f32 v15, v15, v31\n" \
    "v_add_f32 v17, v17, v33\n" \
    "v_max_u32 v253, v253, v241\n" \
    "v_mfma_f32_16x16x32_f16 v[12:15], v[62:65], v[100:103], v[12:15]\n" \
    "ds_read_b128 v[28:31], v53 offset:3072\n" \
    "v_add_f32 v18, v18, v34\n" \
    "v_rcp_f32 v240, v240\n" \
    "v_add_f32 v19, v19, v35\n" \
    "v_mul_f32 v242, v242, v240\n" \
    "v_mul_f32 v243, v243, v240\n" \
    "v_mfma_f32_16x16x32_f16 v[16:19], v[74:77], v[100:103], v[16:19]\n" \
    "ds_read_b128 v[32:35], v53 offset:4096\n" \
    "s_waitcnt lgkmcnt(2)\n" \
    "v_add_f32 v20, v20, v24\n" \
    "v_cvt_pk_bf16_f32 v231, v242, v243\n" \
    "v_add_f32 v21, v21, v25\n" \
    "v_mul_f32 v236, v236, v240\n" \
    "v_add_f32 v22, v22, v26\n" \
    "v_mul_f32 v237, v237, v240\n" \
    "v_add_f32 v23, v23, v27\n" \
    "v_cvt_pk_bf16_f32 v144, v236, v237\n" \
    "v_mul_f32 v238, v238, v240\n" \
    "v_mfma_f32_16x16x32_f16 v[20:23], v[86:89], v[100:103], v[20:23]\n" \
    "ds_read_b128 v[24:27], v53 offset:5120\n" \
    "v_mul_f32 v239, v239, v240\n" \
    "v_cvt_pk_bf16_f32 v145, v238, v239\n" \
    "v_cvt_pk_bf16_f32 v2, v4, v5\n" \
    "v_exp_f32 v7, v7\n" \
    "v_exp_f32 v8, v8\n" \
    "v_cvt_pk_bf16_f32 v3, v6, v7\n" \
    "v_exp_f32 v9, v9\n" \
    "v_exp_f32 v10, v10\n" \
    "v_mfma_f32_16x16x32_bf16 v[248:251], v[164:167], v[0:3], 0\n" \
    "ds_read_b128 v[4:7], v41 offset:22592\n" \
    "v_cvt_pk_bf16_f32 v212, v8, v9\n" \
    "v_mfma_f32_16x16x32_bf16 v[244:247], v[156:159], v[0:3], 0\n" \
    "ds_read_b128 v[0:3], v41 offset:21568\n" \
    "v_exp_f32 v11, v11\n" \
    "v_exp_f32 v12, v12\n" \
    "v_cvt_pk_bf16_f32 v213, v10, v11\n" \
    "v_exp_f32 v13, v13\n" \
    "v_exp_f32 v14, v14\n" \
    "v_mfma_f32_16x16x32_bf16 v[248:251], v[168:171], v[212:215], v[248:251]\n" \
    "ds_read_b128 v[8:11], v41 offset:23616\n" \
    "v_cvt_pk_bf16_f32 v12, v12, v13\n" \
    "v_mfma_f32_16x16x32_bf16 v[244:247], v[160:163], v[212:215], v[244:247]\n" \
    "v_exp_f32 v15, v15\n" \
    "v_exp_f32 v16, v16\n" \
    "v_cvt_pk_bf16_f32 v13, v14, v15\n" \
    "v_exp_f32 v17, v17\n" \
    "v_exp_f32 v18, v18\n" \
    "s_waitcnt lgkmcnt(2)\n" \
    "v_add_f32 v4, v4, v32\n" \
    "v_min_u32 v252, v252, v249\n" \
    "s_waitcnt lgkmcnt(1)\n" \
    "v_add_f32 v0, v0, v28\n" \
    "v_add_f32 v1, v1, v29\n" \
    "v_add_f32 v2, v2, v30\n" \
    "v_add_f32 v3, v3, v31\n" \
    "v_add_f32 v5, v5, v33\n" \
    "v_max_u32 v253, v253, v249\n" \
    "v_mfma_f32_16x16x32_f16 v[0:3], v[62:65], v[116:119], v[0:3]\n" \
    "ds_read_b128 v[28:31], v53 offset:6144\n" \
    "v_add_f32 v6, v6, v34\n" \
    "v_rcp_f32 v248, v248\n" \
    "v_add_f32 v7, v7, v35\n" \
    "v_mul_f32 v250, v250, v248\n" \
    "v_mul_f32 v251, v251, v248\n" \
    "v_mfma_f32_16x16x32_f16 v[4:7], v[74:77], v[116:119], v[4:7]\n" \
    "ds_read_b128 v[32:35], v53 offset:7168\n" \
    "s_waitcnt lgkmcnt(2)\n" \
    "v_add_f32 v8, v8, v24\n" \
    "v_cvt_pk_bf16_f32 v235, v250, v251\n" \
    "v_add_f32 v9, v9, v25\n" \
    "v_mul_f32 v244, v244, v248\n" \
    "v_add_f32 v10, v10, v26\n" \
    "v_mul_f32 v245, v245, v248\n" \
    "v_add_f32 v11, v11, v27\n" \
    "v_cvt_pk_bf16_f32 v148, v244, v245\n" \
    "v_mul_f32 v246, v246, v248\n" \
    "v_mfma_f32_16x16x32_f16 v[8:11], v[86:89], v[116:119], v[8:11]\n" \
    "ds_read_b128 v[24:27], v53 offset:8192\n" \
    "v_mul_f32 v247, v247, v248\n" \
    "v_mfma_f32_16x16x32_bf16 v[248:251], v[180:183], v[220:223], 0\n" \
    "v_cvt_pk_bf16_f32 v149, v246, v247\n" \
    "v_cvt_pk_bf16_f32 v14, v16, v17\n" \
    "v_mfma_f32_16x16x32_bf16 v[244:247], v[172:175], v[220:223], 0\n" \
    "v_exp_f32 v19, v19\n" \
    "v_mfma_f32_16x16x32_bf16 v[248:251], v[184:187], v[216:219], v[248:251]\n" \
    "v_cvt_pk_bf16_f32 v15, v18, v19\n" \
    "v_exp_f32 v20, v20\n" \
    "v_mfma_f32_16x16x32_bf16 v[244:247], v[176:179], v[216:219], v[244:247]\n" \
    "ds_read_b128 v[16:19], v41 offset:25664\n" \
    "v_exp_f32 v21, v21\n" \
    "v_mfma_f32_16x16x32_bf16 v[240:243], v[180:183], v[12:15], 0\n" \
    "v_cvt_pk_bf16_f32 v204, v20, v21\n" \
    "v_exp_f32 v22, v22\n" \
    "v_mfma_f32_16x16x32_bf16 v[236:239], v[172:175], v[12:15], 0\n" \
    "ds_read_b128 v[12:15], v41 offset:24640\n" \
    "v_exp_f32 v23, v23\n" \
    "v_exp_f32 v0, v0\n" \
    "v_cvt_pk_bf16_f32 v205, v22, v23\n" \
    "v_exp_f32 v1, v1\n" \
    "v_exp_f32 v2, v2\n" \
    "v_mfma_f32_16x16x32_bf16 v[240:243], v[184:187], v[204:207], v[240:243]\n" \
    "ds_read_b128 v[20:23], v41 offset:26688\n" \
    "ds_write_b128 v46, v[244:247] offset:512\n" \
    "v_mfma_f32_16x16x32_bf16 v[236:239], v[176:179], v[204:207], v[236:239]\n" \
    "ds_write_b128 v46, v[248:251] offset:768\n" \
    "s_waitcnt lgkmcnt(3)\n" \
    "v_add_f32 v12, v12, v28\n" \
    "v_add_f32 v13, v13, v29\n" \
    "v_add_f32 v14, v14, v30\n" \
    "v_min_u32 v252, v252, v243\n" \
    "v_max_u32 v253, v253, v243\n" \
    "v_rcp_f32 v242, v242\n" \
    "v_add_f32 v15, v15, v31\n" \
    "v_mul_f32 v236, v236, v242\n" \
    "v_mul_f32 v237, v237, v242\n" \
    "v_mfma_f32_16x16x32_f16 v[12:15], v[62:65], v[132:135], v[12:15]\n" \
    "ds_read_b128 v[28:31], v53 offset:0\n" \
    "v_cvt_pk_bf16_f32 v142, v236, v237\n" \
    "v_mul_f32 v238, v238, v242\n" \
    "v_mul_f32 v239, v239, v242\n" \
    "v_add_f32 v16, v16, v32\n" \
    "v_cvt_pk_bf16_f32 v143, v238, v239\n" \
    "v_add_f32 v17, v17, v33\n" \
    "v_mul_f32 v240, v240, v242\n" \
    "v_add_f32 v18, v18, v34\n" \
    "v_mul_f32 v241, v241, v242\n" \
    "v_add_f32 v19, v19, v35\n" \
    "v_cvt_pk_bf16_f32 v156, v240, v241\n" \
    "s_waitcnt lgkmcnt(1)\n" \
    "v_add_f32 v20, v20, v24\n" \
    "v_mfma_f32_16x16x32_f16 v[16:19], v[74:77], v[132:135], v[16:19]\n" \
    "ds_read_b128 v[32:35], v53 offset:1024\n" \
    "v_add_f32 v21, v21, v25\n" \
    "v_add_f32 v22, v22, v26\n" \
    "v_add_f32 v23, v23, v27\n" \
    "v_min_u32 v252, v252, v251\n" \
    "v_max_u32 v253, v253, v251\n" \
    "v_mfma_f32_16x16x32_f16 v[20:23], v[86:89], v[132:135], v[20:23]\n" \
    "ds_read_b128 v[24:27], v53 offset:2048\n" \
    "v_cvt_pk_bf16_f32 v0, v0, v1\n" \
    "v_exp_f32 v3, v3\n" \
    "v_exp_f32 v4, v4\n" \
    "v_cvt_pk_bf16_f32 v1, v2, v3\n" \
    "v_exp_f32 v5, v5\n" \
    "v_exp_f32 v6, v6\n" \
    "v_cvt_pk_bf16_f32 v2, v4, v5\n" \
    "v_exp_f32 v7, v7\n" \
    "v_exp_f32 v8, v8\n" \
    "v_cvt_pk_bf16_f32 v3, v6, v7\n" \
    "v_exp_f32 v9, v9\n" \
    "v_exp_f32 v10, v10\n" \
    "v_mfma_f32_16x16x32_bf16 v[240:243], v[180:183], v[0:3], 0\n" \
    "ds_read_b128 v[4:7], v41 offset:28768\n" \
    "v_cvt_pk_bf16_f32 v208, v8, v9\n" \
    "v_mfma_f32_16x16x32_bf16 v[236:239], v[172:175], v[0:3], 0\n" \
    "ds_read_b128 v[0:3], v41 offset:27744\n" \
    "v_exp_f32 v11, v11\n" \
    "v_exp_f32 v12, v12\n" \
    "v_cvt_pk_bf16_f32 v209, v10, v11\n" \
    "v_exp_f32 v13, v13\n" \
    "v_exp_f32 v14, v14\n" \
    "v_mfma_f32_16x16x32_bf16 v[240:243], v[184:187], v[208:211], v[240:243]\n" \
    "ds_read_b128 v[8:11], v41 offset:29792\n" \
    "v_cvt_pk_bf16_f32 v12, v12, v13\n" \
    "v_mfma_f32_16x16x32_bf16 v[236:239], v[176:179], v[208:211], v[236:239]\n" \
    "v_exp_f32 v15, v15\n" \
    "v_exp_f32 v16, v16\n" \
    "v_cvt_pk_bf16_f32 v13, v14, v15\n" \
    "v_exp_f32 v17, v17\n" \
    "v_exp_f32 v18, v18\n" \
    "s_waitcnt lgkmcnt(2)\n" \
    "v_add_f32 v4, v4, v32\n" \
    "v_min_u32 v252, v252, v243\n" \
    "s_waitcnt lgkmcnt(1)\n" \
    "v_add_f32 v0, v0, v28\n" \
    "v_add_f32 v1, v1, v29\n" \
    "v_add_f32 v2, v2, v30\n" \
    "v_add_f32 v3, v3, v31\n" \
    "v_add_f32 v5, v5, v33\n" \
    "v_max_u32 v253, v253, v243\n" \
    "v_mfma_f32_16x16x32_f16 v[0:3], v[64:67], v[104:107], v[0:3]\n" \
    "ds_read_b128 v[28:31], v53 offset:3072\n" \
    "v_add_f32 v6, v6, v34\n" \
    "v_rcp_f32 v242, v242\n" \
    "v_add_f32 v7, v7, v35\n" \
    "v_mul_f32 v236, v236, v242\n" \
    "v_mul_f32 v237, v237, v242\n" \
    "v_mfma_f32_16x16x32_f16 v[4:7], v[76:79], v[104:107], v[4:7]\n" \
    "ds_read_b128 v[32:35], v53 offset:4096\n" \
    "s_waitcnt lgkmcnt(2)\n" \
    "v_add_f32 v8, v8, v24\n" \
    "v_cvt_pk_bf16_f32 v146, v236, v237\n" \
    "v_add_f32 v9, v9, v25\n" \
    "v_mul_f32 v238, v238, v242\n" \
    "v_add_f32 v10, v10, v26\n" \
    "v_mul_f32 v239, v239, v242\n" \
    "v_add_f32 v11, v11, v27\n" \
    "v_cvt_pk_bf16_f32 v147, v238, v239\n" \
    "v_mul_f32 v240, v240, v242\n" \
    "v_mfma_f32_16x16x32_f16 v[8:11], v[88:91], v[104:107], v[8:11]\n" \
    "ds_read_b128 v[24:27], v53 offset:5120\n" \
    "v_mul_f32 v241, v241, v242\n" \
    "v_cvt_pk_bf16_f32 v160, v240, v241\n" \
    "v_cvt_pk_bf16_f32 v14, v16, v17\n" \
    "v_exp_f32 v19, v19\n" \
    "v_exp_f32 v20, v20\n" \
    "v_cvt_pk_bf16_f32 v15, v18, v19\n" \
    "v_exp_f32 v21, v21\n" \
    "v_exp_f32 v22, v22\n" \
    "v_mfma_f32_16x16x32_bf16 v[248:251], v[180:183], v[12:15], 0\n" \
    "ds_read_b128 v[16:19], v41 offset:31840\n" \
    "v_cvt_pk_bf16_f32 v212, v20, v21\n" \
    "v_mfma_f32_16x16x32_bf16 v[244:247], v[172:175], v[12:15], 0\n" \
    "ds_read_b128 v[12:15], v41 offset:30816\n" \
    "v_exp_f32 v23, v23\n" \
    "v_exp_f32 v0, v0\n" \
    "v_cvt_pk_bf16_f32 v213, v22, v23\n" \
    "v_exp_f32 v1, v1\n" \
    "v_exp_f32 v2, v2\n" \
    "v_mfma_f32_16x16x32_bf16 v[248:251], v[184:187], v[212:215], v[248:251]\n" \
    "ds_read_b128 v[20:23], v41 offset:32864\n" \
    "v_cvt_pk_bf16_f32 v0, v0, v1\n" \
    "v_mfma_f32_16x16x32_bf16 v[244:247], v[176:179], v[212:215], v[244:247]\n" \
    "v_exp_f32 v3, v3\n" \
    "v_exp_f32 v4, v4\n" \
    "v_cvt_pk_bf16_f32 v1, v2, v3\n" \
    "v_exp_f32 v5, v5\n" \
    "v_exp_f32 v6, v6\n" \
    "s_waitcnt lgkmcnt(2)\n" \
    "v_add_f32 v16, v16, v32\n" \
    "v_min_u32 v252, v252, v251\n" \
    "s_waitcnt lgkmcnt(1)\n" \
    "v_add_f32 v12, v12, v28\n" \
    "v_add_f32 v13, v13, v29\n" \
    "v_add_f32 v14, v14, v30\n" \
    "v_add_f32 v15, v15, v31\n" \
    "v_add_f32 v17, v17, v33\n" \
    "v_max_u32 v253, v253, v251\n" \
    "v_mfma_f32_16x16x32_f16 v[12:15], v[64:67], v[120:123], v[12:15]\n" \
    "ds_read_b128 v[28:31], v53 offset:6144\n" \
    "v_add_f32 v18, v18, v34\n" \
    "v_rcp_f32 v250, v250\n" \
    "v_add_f32 v19, v19, v35\n" \
    "v_mul_f32 v244, v244, v250\n" \
    "v_mul_f32 v245, v245, v250\n" \
    "v_mfma_f32_16x16x32_f16 v[16:19], v[76:79], v[120:123], v[16:19]\n" \
    "ds_read_b128 v[32:35], v53 offset:7168\n" \
    "s_waitcnt lgkmcnt(2)\n" \
    "v_add_f32 v20, v20, v24\n" \
    "v_cvt_pk_bf16_f32 v150, v244, v245\n" \
    "v_add_f32 v21, v21, v25\n" \
    "v_mul_f32 v246, v246, v250\n" \
    "v_add_f32 v22, v22, v26\n" \
    "v_mul_f32 v247, v247, v250\n" \
    "v_add_f32 v23, v23, v27\n" \
    "v_cvt_pk_bf16_f32 v151, v246, v247\n" \
    "v_mfma_f32_16x16x32_bf16 v[244:247], v[188:191], v[220:223], 0\n" \
    "ds_read_b128 v[24:27], v53 offset:8192\n" \
    "v_mul_f32 v248, v248, v250\n" \
    "v_mfma_f32_16x16x32_f16 v[20:23], v[88:91], v[120:123], v[20:23]\n" \
    "v_mul_f32 v249, v249, v250\n" \
    "v_cvt_pk_bf16_f32 v164, v248, v249\n" \
    "v_mfma_f32_16x16x32_bf16 v[248:251], v[196:199], v[220:223], 0\n" \
    "v_cvt_pk_bf16_f32 v2, v4, v5\n" \
    "v_exp_f32 v7, v7\n" \
    "v_mfma_f32_16x16x32_bf16 v[248:251], v[200:203], v[216:219], v[248:251]\n" \
    "v_cvt_pk_bf16_f32 v3, v6, v7\n" \
    "v_exp_f32 v8, v8\n" \
    "v_mfma_f32_16x16x32_bf16 v[244:247], v[192:195], v[216:219], v[244:247]\n" \
    "ds_read_b128 v[4:7], v41 offset:34912\n" \
    "v_exp_f32 v9, v9\n" \
    "v_mfma_f32_16x16x32_bf16 v[240:243], v[196:199], v[0:3], 0\n" \
    "v_cvt_pk_bf16_f32 v204, v8, v9\n" \
    "v_exp_f32 v10, v10\n" \
    "v_mfma_f32_16x16x32_bf16 v[236:239], v[188:191], v[0:3], 0\n" \
    "ds_read_b128 v[0:3], v41 offset:33888\n" \
    "v_exp_f32 v11, v11\n" \
    "v_exp_f32 v12, v12\n" \
    "v_cvt_pk_bf16_f32 v205, v10, v11\n" \
    "v_exp_f32 v13, v13\n" \
    "v_exp_f32 v14, v14\n" \
    "v_mfma_f32_16x16x32_bf16 v[240:243], v[200:203], v[204:207], v[240:243]\n" \
    "ds_read_b128 v[8:11], v41 offset:35936\n" \
    "ds_write_b128 v47, v[244:247] offset:512\n" \
    "v_mfma_f32_16x16x32_bf16 v[236:239], v[192:195], v[204:207], v[236:239]\n" \
    "ds_write_b128 v47, v[248:251] offset:768\n" \
    "s_waitcnt lgkmcnt(3)\n" \
    "v_add_f32 v0, v0, v28\n" \
    "v_add_f32 v1, v1, v29\n" \
    "v_add_f32 v2, v2, v30\n" \
    "v_min_u32 v252, v252, v241\n" \
    "v_max_u32 v253, v253, v241\n" \
    "v_rcp_f32 v240, v240\n" \
    "v_add_f32 v3, v3, v31\n" \
    "v_mul_f32 v242, v242, v240\n" \
    "v_mul_f32 v243, v243, v240\n" \
    "v_mfma_f32_16x16x32_f16 v[0:3], v[64:67], v[136:139], v[0:3]\n" \
    "v_cvt_pk_bf16_f32 v157, v242, v243\n" \
    "v_mul_f32 v236, v236, v240\n" \
    "v_mul_f32 v237, v237, v240\n" \
    "v_add_f32 v4, v4, v32\n" \
    "v_cvt_pk_bf16_f32 v158, v236, v237\n" \
    "v_add_f32 v5, v5, v33\n" \
    "v_mul_f32 v238, v238, v240\n" \
    "v_add_f32 v6, v6, v34\n" \
    "v_mul_f32 v239, v239, v240\n" \
    "v_add_f32 v7, v7, v35\n" \
    "v_cvt_pk_bf16_f32 v159, v238, v239\n" \
    "s_waitcnt lgkmcnt(0)\n" \
    "v_add_f32 v8, v8, v24\n" \
    "v_mfma_f32_16x16x32_f16 v[4:7], v[76:79], v[136:139], v[4:7]\n" \
    "v_add_f32 v9, v9, v25\n" \
    "v_add_f32 v10, v10, v26\n" \
    "v_add_f32 v11, v11, v27\n" \
    "v_min_u32 v252, v252, v249\n" \
    "v_max_u32 v253, v253, v249\n" \
    "v_mfma_f32_16x16x32_f16 v[8:11], v[88:91], v[136:139], v[8:11]\n" \
    "v_cvt_pk_bf16_f32 v12, v12, v13\n" \
    "v_exp_f32 v15, v15\n" \
    "v_exp_f32 v16, v16\n" \
    "v_cvt_pk_bf16_f32 v13, v14, v15\n" \
    "v_exp_f32 v17, v17\n" \
    "v_exp_f32 v18, v18\n" \
    "v_cvt_pk_bf16_f32 v14, v16, v17\n" \
    "v_exp_f32 v19, v19\n" \
    "v_exp_f32 v20, v20\n" \
    "v_cvt_pk_bf16_f32 v15, v18, v19\n" \
    "v_exp_f32 v21, v21\n" \
    "v_exp_f32 v22, v22\n" \
    "v_mfma_f32_16x16x32_bf16 v[240:243], v[196:199], v[12:15], 0\n" \
    "v_cvt_pk_bf16_f32 v208, v20, v21\n" \
    "v_exp_f32 v23, v23\n" \
    "v_mfma_f32_16x16x32_bf16 v[236:239], v[188:191], v[12:15], 0\n" \
    "v_cvt_pk_bf16_f32 v209, v22, v23\n" \
    "v_mov_b32 v220, 148752\n" \
    "v_mov_b32 v221, 1\n" \
    "v_mfma_f32_16x16x32_bf16 v[240:243], v[200:203], v[208:211], v[240:243]\n" \
    "v_exp_f32 v0, v0\n" \
    "v_mfma_f32_16x16x32_bf16 v[236:239], v[192:195], v[208:211], v[236:239]\n" \
    "v_exp_f32 v1, v1\n" \
    "v_exp_f32 v2, v2\n" \
    "v_cvt_pk_bf16_f32 v0, v0, v1\n" \
    "v_exp_f32 v3, v3\n" \
    "v_exp_f32 v4, v4\n" \
    "v_cvt_pk_bf16_f32 v1, v2, v3\n" \
    "v_min_u32 v252, v252, v241\n" \
    "v_max_u32 v253, v253, v241\n" \
    "v_rcp_f32 v240, v240\n" \
    "v_exp_f32 v5, v5\n" \
    "v_mul_f32 v242, v242, v240\n" \
    "v_mul_f32 v243, v243, v240\n" \
    "v_cvt_pk_bf16_f32 v161, v242, v243\n" \
    "v_mul_f32 v236, v236, v240\n" \
    "v_cvt_pk_bf16_f32 v2, v4, v5\n" \
    "v_mul_f32 v237, v237, v240\n" \
    "v_exp_f32 v6, v6\n" \
    "v_cvt_pk_bf16_f32 v162, v236, v237\n" \
    "v_exp_f32 v7, v7\n" \
    "v_mul_f32 v238, v238, v240\n" \
    "v_cvt_pk_bf16_f32 v3, v6, v7\n" \
    "v_mul_f32 v239, v239, v240\n" \
    "v_exp_f32 v8, v8\n" \
    "v_mfma_f32_16x16x32_bf16 v[248:251], v[196:199], v[0:3], 0\n" \
    "v_cvt_pk_bf16_f32 v163, v238, v239\n" \
    "v_exp_f32 v9, v9\n" \
    "v_mfma_f32_16x16x32_bf16 v[244:247], v[188:191], v[0:3], 0\n" \
    "v_cvt_pk_bf16_f32 v212, v8, v9\n" \
    "v_exp_f32 v10, v10\n" \
    "v_exp_f32 v11, v11\n" \
    "v_add_u32 v44, 8256, v44\n" \
    "v_cvt_pk_bf16_f32 v213, v10, v11\n" \
    "v_add_u32 v45, 8256, v45\n" \
    "v_add_u32 v46, 8256, v46\n" \
    "v_mfma_f32_16x16x32_bf16 v[248:251], v[200:203], v[212:215], v[248:251]\n" \
    "v_add_u32 v47, 8256, v47\n" \
    "v_add_u32 v54, 8256, v54\n" \
    "v_mfma_f32_16x16x32_bf16 v[244:247], v[192:195], v[212:215], v[244:247]\n" \
    "s_nop 4\n" \
    "v_min_u32 v252, v252, v249\n" \
    "v_max_u32 v253, v253, v249\n" \
    "v_sub_u32 v252, v253, v252\n" \
    "v_cmp_ne_u32 vcc, 0x4b000000, v252\n" \
    "v_rcp_f32 v248, v248\n" \
    "s_cbranch_vccnz Lexit%=\n" \
    "s_mov_b64 exec, 1\n" \
    "ds_add_u32 v220, v221\n" \
    "s_mov_b64 exec, -1\n" \
    "s_cmp_lg_u32 s55, 0\n" \
    "s_cbranch_scc1 Lh2m%=\n" \
    "Lr2m%=:\n" \
    "ds_read_b128 v[172:175], v40\n" \
    "ds_read_b128 v[176:179], v40 offset:1024\n" \
    "v_mul_f32 v250, v250, v248\n" \
    "v_mul_f32 v251, v251, v248\n" \
    "v_cvt_pk_bf16_f32 v165, v250, v251\n" \
    "v_mul_f32 v244, v244, v248\n" \
    "v_mul_f32 v245, v245, v248\n" \
    "v_cvt_pk_bf16_f32 v166, v244, v245\n" \
    "v_mul_f32 v246, v246, v248\n" \
    "v_mul_f32 v247, v247, v248\n" \
    "v_cvt_pk_bf16_f32 v167, v246, v247\n" \
    "ds_read_b128 v[180:183], v40 offset:2048\n" \
    "ds_read_b128 v[184:187], v40 offset:3072\n" \
    "s_waitcnt lgkmcnt(3)\n" \
    "v_mfma_f32_16x16x32_bf16 v[60:63], v[172:175], v[224:227], 0\n" \
    "v_mfma_f32_16x16x32_bf16 v[64:67], v[172:175], v[228:231], 0\n" \
    "ds_read_b128 v[172:175], v40 offset:4096\n" \
    "s_waitcnt lgkmcnt(3)\n" \
    "v_mfma_f32_16x16x32_bf16 v[60:63], v[176:179], v[140:143], v[60:63]\n" \
    "v_mfma_f32_16x16x32_bf16 v[64:67], v[176:179], v[144:147], v[64:67]\n" \
    "ds_read_b128 v[176:179], v40 offset:5120\n" \
    "s_waitcnt lgkmcnt(2)\n" \
    "v_mfma_f32_16x16x32_bf16 v[60:63], v[180:183], v[156:159], v[60:63]\n" \
    "v_mfma_f32_16x16x32_bf16 v[64:67], v[180:183], v[160:163], v[64:67]\n" \
    "ds_read_b128 v[180:183], v40 offset:6144\n" \
    "v_mfma_f32_16x16x32_bf16 v[72:75], v[184:187], v[224:227], 0\n" \
    "v_mfma_f32_16x16x32_bf16 v[76:79], v[184:187], v[228:231], 0\n" \
    "ds_read_b128 v[184:187], v40 offset:7168\n" \
    "s_waitcnt lgkmcnt(3)\n" \
    "v_mfma_f32_16x16x32_bf16 v[72:75], v[172:175], v[140:143], v[72:75]\n" \
    "s_nop 0\n" \
    "v_fmaak_f32 v152, v60, v60, 0x400d8ec9\n" \
    "v_mfma_f32_16x16x32_bf16 v[76:79], v[172:175], v[144:147], v[76:79]\n" \
    "ds_read_b128 v[172:175], v40 offset:8192\n" \
    "v_fmac_f32 v152, v61, v61\n" \
    "s_waitcnt lgkmcnt(3)\n" \
    "v_mfma_f32_16x16x32_bf16 v[72:75], v[176:179], v[156:159], v[72:75]\n" \
    "v_fmac_f32 v152, v62, v62\n" \
    "v_fmac_f32 v152, v63, v63\n" \
    "v_mfma_f32_16x16x32_bf16 v[76:79], v[176:179], v[160:163], v[76:79]\n" \
    "ds_read_b128 v[176:179], v40 offset:9216\n" \
    "v_fmaak_f32 v153, v64, v64, 0x400d8ec9\n" \
    "s_waitcnt lgkmcnt(3)\n" \
    "v_mfma_f32_16x16x32_bf16 v[84:87], v[180:183], v[224:227], 0\n" \
    "v_fmac_f32 v153, v65, v65\n" \
    "v_fmac_f32 v152, v72, v72\n" \
    "v_mfma_f32_16x16x32_bf16 v[88:91], v[180:183], v[228:231], 0\n" \
    "ds_read_b128 v[180:183], v40 offset:10240\n" \
    "v_fmac_f32 v152, v73, v73\n" \
    "s_waitcnt lgkmcnt(3)\n" \
    "v_mfma_f32_16x16x32_bf16 v[84:87], v[184:187], v[140:143], v[84:87]\n" \
    "v_fmac_f32 v152, v74, v74\n" \
    "v_fmac_f32 v152, v75, v75\n" \
    "v_mfma_f32_16x16x32_bf16 v[88:91], v[184:187], v[144:147], v[88:91]\n" \
    "ds_read_b128 v[184:187], v40 offset:11264\n" \
    "v_fmac_f32 v153, v66, v66\n" \
    "s_waitcnt lgkmcnt(3)\n" \
    "v_mfma_f32_16x16x32_bf16 v[84:87], v[172:175], v[156:159], v[84:87]\n" \
    "v_fmac_f32 v153, v67, v67\n" \
    "v_fmac_f32 v153, v76, v76\n" \
    "v_mfma_f32_16x16x32_bf16 v[88:91], v[172:175], v[160:163], v[88:91]\n" \
    "ds_read_b128 v[172:175], v40 offset:12288\n" \
    "v_fmac_f32 v153, v77, v77\n" \
    "s_waitcnt lgkmcnt(3)\n" \
    "v_mfma_f32_16x16x32_bf16 v[56:59], v[176:179], v[224:227], 0\n" \
    "v_fmac_f32 v153, v78, v78\n" \
    "v_fmac_f32 v152, v84, v84\n" \
    "v_mfma_f32_16x16x32_bf16 v[68:71], v[176:179], v[228:231], 0\n" \
    "ds_read_b128 v[176:179], v40 offset:13312\n" \
    "v_fmac_f32 v152, v85, v85\n" \
    "s_waitcnt lgkmcnt(3)\n" \
    "v_mfma_f32_16x16x32_bf16 v[56:59], v[180:183], v[140:143], v[56:59]\n" \
    "v_fmac_f32 v152, v86, v86\n" \
    "v_fmac_f32 v152, v87, v87\n" \
    "v_mfma_f32_16x16x32_bf16 v[68:71], v[180:183], v[144:147], v[68:71]\n" \
    "ds_read_b128 v[180:183], v40 offset:14336\n" \
    "v_fmac_f32 v153, v79, v79\n" \
    "s_waitcnt lgkmcnt(3)\n" \
    "v_mfma_f32_16x16x32_bf16 v[56:59], v[184:187], v[156:159], v[56:59]\n" \
    "v_fmac_f32 v153, v88, v88\n" \
    "v_fmac_f32 v153, v89, v89\n" \
    "v_mfma_f32_16x16x32_bf16 v[68:71], v[184:187], v[160:163], v[68:71]\n" \
    "ds_read_b128 v[184:187], v40 offset:15360\n" \
    "v_fmac_f32 v153, v90, v90\n" \
    "s_waitcnt lgkmcnt(3)\n" \
    "v_mfma_f32_16x16x32_bf16 v[80:83], v[172:175], v[224:227], 0\n" \
    "v_fmac_f32 v153, v91, v91\n" \
    "v_fmac_f32 v152, v56, v56\n" \
    "v_mfma_f32_16x16x32_bf16 v[204:207], v[172:175], v[228:231], 0\n" \
    "ds_read_b128 v[172:175], v40 offset:16384\n" \
    "v_fmac_f32 v152, v57, v57\n" \
    "s_waitcnt lgkmcnt(3)\n" \
    "v_mfma_f32_16x16x32_bf16 v[80:83], v[176:179], v[140:143], v[80:83]\n" \
    "v_fmac_f32 v152, v58, v58\n" \
    "v_fmac_f32 v152, v59, v59\n" \
    "v_mfma_f32_16x16x32_bf16 v[204:207], v[176:179], v[144:147], v[204:207]\n" \
    "ds_read_b128 v[176:179], v40 offset:17408\n" \
    "v_fmac_f32 v153, v68, v68\n" \
    "s_waitcnt lgkmcnt(3)\n" \
    "v_mfma_f32_16x16x32_bf16 v[80:83], v[180:183], v[156:159], v[80:83]\n" \
    "v_fmac_f32 v153, v69, v69\n" \
    "v_fmac_f32 v153, v70, v70\n" \
    "v_mfma_f32_16x16x32_bf16 v[204:207], v[180:183], v[160:163], v[204:207]\n" \
    "ds_read_b128 v[180:183], v40\n" \
    "v_fmac_f32 v153, v71, v71\n" \
    "s_waitcnt lgkmcnt(3)\n" \
    "v_mfma_f32_16x16x32_bf16 v[208:211], v[184:187], v[224:227], 0\n" \
    "s_nop 0\n" \
    "v_fmac_f32 v152, v80, v80\n" \
    "v_mfma_f32_16x16x32_bf16 v[220:223], v[184:187], v[228:231], 0\n" \
    "ds_read_b128 v[184:187], v40 offset:1024\n" \
    "v_fmac_f32 v152, v81, v81\n" \
    "s_waitcnt lgkmcnt(3)\n" \
    "v_mfma_f32_16x16x32_bf16 v[208:211], v[172:175], v[140:143], v[208:211]\n" \
    "v_fmac_f32 v152, v82, v82\n" \
    "v_fmac_f32 v152, v83, v83\n" \
    "v_mfma_f32_16x16x32_bf16 v[220:223], v[172:175], v[144:147], v[220:223]\n" \
    "ds_read_b128 v[172:175], v40 offset:2048\n" \
    "v_fmac_f32 v153, v204, v204\n" \
    "s_waitcnt lgkmcnt(3)\n" \
    "v_mfma_f32_16x16x32_bf16 v[208:211], v[176:179], v[156:159], v[208:211]\n" \
    "v_fmac_f32 v153, v205, v205\n" \
    "v_fmac_f32 v153, v206, v206\n" \
    "v_mfma_f32_16x16x32_bf16 v[220:223], v[176:179], v[160:163], v[220:223]\n" \
    "ds_read_b128 v[176:179], v40 offset:3072\n" \
    "v_fmac_f32 v153, v207, v207\n" \
    "s_waitcnt lgkmcnt(3)\n" \
    "v_mfma_f32_16x16x32_bf16 v[216:219], v[180:183], v[232:235], 0\n" \
    "ds_read_b128 v[180:183], v40 offset:4096\n" \
    "v_fmac_f32 v152, v208, v208\n" \
    "v_fmac_f32 v152, v209, v209\n" \
    "v_fmac_f32 v152, v210, v210\n" \
    "v_fmac_f32 v152, v211, v211\n" \
    "s_waitcnt lgkmcnt(3)\n" \
    "v_mfma_f32_16x16x32_bf16 v[216:219], v[184:187], v[148:151], v[216:219]\n" \
    "ds_read_b128 v[184:187], v40 offset:5120\n" \
    "v_mov_b32 v155, v152\n" \
    "v_fmac_f32 v153, v220, v220\n" \
    "v_fmac_f32 v153, v221, v221\n" \
    "v_permlane32_swap_b32 v152, v155\n" \
    "s_waitcnt lgkmcnt(3)\n" \
    "v_mfma_f32_16x16x32_bf16 v[216:219], v[172:175], v[164:167], v[216:219]\n" \
    "ds_read_b128 v[172:175], v40 offset:6144\n" \
    "v_add_f32 v152, v152, v155\n" \
    "v_mov_b32 v155, v152\n" \
    "v_fmac_f32 v153, v222, v222\n" \
    "v_fmac_f32 v153, v223, v223\n" \
    "v_permlane16_swap_b32 v152, v155\n" \
    "s_waitcnt lgkmcnt(3)\n" \
    "v_mfma_f32_16x16x32_bf16 v[212:215], v[176:179], v[232:235], 0\n" \
    "ds_read_b128 v[176:179], v40 offset:7168\n" \
    "v_add_f32 v152, v152, v155\n" \
    "s_waitcnt lgkmcnt(3)\n" \
    "v_mfma_f32_16x16x32_bf16 v[212:215], v[180:183], v[148:151], v[212:215]\n" \
    "ds_read_b128 v[180:183], v40 offset:8192\n" \
    "v_mul_f32 v152, 0x3c2aaaab, v152\n" \
    "v_rsq_f32 v168, v152\n" \
    "s_waitcnt lgkmcnt(3)\n" \
    "v_mfma_f32_16x16x32_bf16 v[212:215], v[184:187], v[164:167], v[212:215]\n" \
    "ds_read_b128 v[184:187], v40 offset:9216\n" \
    "v_mul_f32 v60, v60, v168\n" \
    "v_mul_f32 v61, v61, v168\n" \
    "v_cvt_pk_bf16_f32 v60, v60, v61\n" \
    "v_mul_f32 v62, v62, v168\n" \
    "v_mul_f32 v63, v63, v168\n" \
    "s_waitcnt lgkmcnt(3)\n" \
    "v_mfma_f32_16x16x32_bf16 v[236:239], v[172:175], v[232:235], 0\n" \
    "ds_read_b128 v[172:175], v40 offset:10240\n" \
    "v_cvt_pk_bf16_f32 v61, v62, v63\n" \
    "v_mul_f32 v72, v72, v168\n" \
    "v_mul_f32 v73, v73, v168\n" \
    "v_cvt_pk_bf16_f32 v62, v72, v73\n" \
    "v_mul_f32 v74, v74, v168\n" \
    "v_mul_f32 v75, v75, v168\n" \
    "s_waitcnt lgkmcnt(3)\n" \
    "v_mfma_f32_16x16x32_bf16 v[236:239], v[176:179], v[148:151], v[236:239]\n" \
    "ds_read_b128 v[176:179], v40 offset:11264\n" \
    "v_cvt_pk_bf16_f32 v63, v74, v75\n" \
    "s_waitcnt lgkmcnt(3)\n" \
    "v_mfma_f32_16x16x32_bf16 v[236:239], v[180:183], v[164:167], v[236:239]\n" \
    "buffer_store_dwordx4 v[60:63], v36, s[48:51], s56 offen sc0 sc1\n" \
    "s_waitcnt lgkmcnt(2)\n" \
    "v_mfma_f32_16x16x32_bf16 v[240:243], v[184:187], v[232:235], 0\n" \
    "ds_read_b128 v[180:183], v40 offset:12288\n" \
    "ds_read_b128 v[184:187], v40 offset:13312\n" \
    "v_mul_f32 v84, v84, v168\n" \
    "v_mul_f32 v85, v85, v168\n" \
    "v_cvt_pk_bf16_f32 v84, v84, v85\n" \
    "s_waitcnt lgkmcnt(3)\n" \
    "v_mfma_f32_16x16x32_bf16 v[240:243], v[172:175], v[148:151], v[240:243]\n" \
    "ds_read_b128 v[172:175], v40 offset:14336\n" \
    "v_mul_f32 v86, v86, v168\n" \
    "v_mul_f32 v87, v87, v168\n" \
    "v_cvt_pk_bf16_f32 v85, v86, v87\n" \
    "v_mul_f32 v56, v56, v168\n" \
    "v_mul_f32 v57, v57, v168\n" \
    "v_cvt_pk_bf16_f32 v86, v56, v57\n" \
    "s_waitcnt lgkmcnt(3)\n" \
    "v_mfma_f32_16x16x32_bf16 v[240:243], v[176:179], v[164:167], v[240:243]\n" \
    "ds_read_b128 v[176:179], v40 offset:15360\n" \
    "v_mul_f32 v58, v58, v168\n" \
    "v_mul_f32 v59, v59, v168\n" \
    "v_cvt_pk_bf16_f32 v87, v58, v59\n" \
    "v_mul_f32 v80, v80, v168\n" \
    "v_mul_f32 v81, v81, v168\n" \
    "v_cvt_pk_bf16_f32 v80, v80, v81\n" \
    "v_mul_f32 v82, v82, v168\n" \
    "v_mul_f32 v83, v83, v168\n" \
    "v_cvt_pk_bf16_f32 v81, v82, v83\n" \
    "s_waitcnt lgkmcnt(3)\n" \
    "v_mfma_f32_16x16x32_bf16 v[244:247], v[180:183], v[232:235], 0\n" \
    "buffer_store_dwordx4 v[84:87], v36, s[48:51], s56 offen offset:64 sc0 sc1\n" \
    "s_waitcnt lgkmcnt(1)\n" \
    "v_mfma_f32_16x16x32_bf16 v[244:247], v[184:187], v[148:151], v[244:247]\n" \
    "ds_read_b128 v[180:183], v40 offset:16384\n" \
    "ds_read_b128 v[184:187], v40 offset:17408\n" \
    "v_mfma_f32_16x16x32_bf16 v[244:247], v[172:175], v[164:167], v[244:247]\n" \
    "v_mul_f32 v208, v208, v168\n" \
    "v_mul_f32 v209, v209, v168\n" \
    "v_cvt_pk_bf16_f32 v82, v208, v209\n" \
    "s_waitcnt lgkmcnt(2)\n" \
    "v_mfma_f32_16x16x32_bf16 v[248:251], v[176:179], v[232:235], 0\n" \
    "v_mul_f32 v210, v210, v168\n" \
    "v_mul_f32 v211, v211, v168\n" \
    "v_cvt_pk_bf16_f32 v83, v210, v211\n" \
    "v_mov_b32 v155, v153\n" \
    "buffer_store_dwordx4 v[80:83], v36, s[48:51], s56 offen offset:128 sc0 sc1\n" \
    "v_fmaak_f32 v154, v216, v216, 0x400d8ec9\n" \
    "v_permlane32_swap_b32 v153, v155\n" \
    "v_add_f32 v153, v153, v155\n" \
    "v_mov_b32 v155, v153\n" \
    "v_fmac_f32 v154, v217, v217\n" \
    "v_fmac_f32 v154, v218, v218\n" \
    "s_waitcnt lgkmcnt(1)\n" \
    "v_mfma_f32_16x16x32_bf16 v[248:251], v[180:183], v[148:151], v[248:251]\n" \
    "v_permlane16_swap_b32 v153, v155\n" \
    "s_waitcnt lgkmcnt(0)\n" \
    "v_mfma_f32_16x16x32_bf16 v[248:251], v[184:187], v[164:167], v[248:251]\n" \
    "v_add_f32 v153, v153, v155\n" \
    "v_mul_f32 v153, 0x3c2aaaab, v153\n" \
    "v_rsq_f32 v169, v153\n" \
    "v_fmac_f32 v154, v219, v219\n" \
    "v_mul_f32 v64, v64, v169\n" \
    "v_mul_f32 v65, v65, v169\n" \
    "v_cvt_pk_bf16_f32 v64, v64, v65\n" \
    "v_mul_f32 v66, v66, v169\n" \
    "v_mul_f32 v67, v67, v169\n" \
    "v_cvt_pk_bf16_f32 v65, v66, v67\n" \
    "v_mul_f32 v76, v76, v169\n" \
    "v_mul_f32 v77, v77, v169\n" \
    "v_cvt_pk_bf16_f32 v66, v76, v77\n" \
    "v_mul_f32 v78, v78, v169\n" \
    "v_mul_f32 v79, v79, v169\n" \
    "v_cvt_pk_bf16_f32 v67, v78, v79\n" \
    "v_mul_f32 v88, v88, v169\n" \
    "v_mul_f32 v89, v89, v169\n" \
    "v_cvt_pk_bf16_f32 v88, v88, v89\n" \
    "v_mul_f32 v90, v90, v169\n" \
    "v_mul_f32 v91, v91, v169\n" \
    "v_cvt_pk_bf16_f32 v89, v90, v91\n" \
    "v_mul_f32 v68, v68, v169\n" \
    "v_mul_f32 v69, v69, v169\n" \
    "v_cvt_pk_bf16_f32 v90, v68, v69\n" \
    "v_mul_f32 v70, v70, v169\n" \
    "v_mul_f32 v71, v71, v169\n" \
    "v_cvt_pk_bf16_f32 v91, v70, v71\n" \
    "v_mul_f32 v204, v204, v169\n" \
    "v_mul_f32 v205, v205, v169\n" \
    "v_cvt_pk_bf16_f32 v204, v204, v205\n" \
    "v_mul_f32 v206, v206, v169\n" \
    "v_mul_f32 v207, v207, v169\n" \
    "v_cvt_pk_bf16_f32 v205, v206, v207\n" \
    "v_mul_f32 v220, v220, v169\n" \
    "v_mul_f32 v221, v221, v169\n" \
    "v_cvt_pk_bf16_f32 v206, v220, v221\n" \
    "v_mul_f32 v222, v222, v169\n" \
    "v_mul_f32 v223, v223, v169\n" \
    "v_cvt_pk_bf16_f32 v207, v222, v223\n" \
    "buffer_store_dwordx4 v[64:67], v37, s[48:51], s56 offen sc0 sc1\n" \
    "buffer_store_dwordx4 v[88:91], v37, s[48:51], s56 offen offset:64 sc0 sc1\n" \
    "buffer_store_dwordx4 v[204:207], v37, s[48:51], s56 offen offset:128 sc0 sc1\n" \
    "v_fmac_f32 v154, v212, v212\n" \
    "v_fmac_f32 v154, v213, v213\n" \
    "v_fmac_f32 v154, v214, v214\n" \
    "v_fmac_f32 v154, v215, v215\n" \
    "v_fmac_f32 v154, v236, v236\n" \
    "v_fmac_f32 v154, v237, v237\n" \
    "v_fmac_f32 v154, v238, v238\n" \
    "v_fmac_f32 v154, v239, v239\n" \
    "v_fmac_f32 v154, v240, v240\n" \
    "v_fmac_f32 v154, v241, v241\n" \
    "v_fmac_f32 v154, v242, v242\n" \
    "v_fmac_f32 v154, v243, v243\n" \
    "v_fmac_f32 v154, v244, v244\n" \
    "v_fmac_f32 v154, v245, v245\n" \
    "v_fmac_f32 v154, v246, v246\n" \
    "v_fmac_f32 v154, v247, v247\n" \
    "v_fmac_f32 v154, v248, v248\n" \
    "v_fmac_f32 v154, v249, v249\n" \
    "v_fmac_f32 v154, v250, v250\n" \
    "v_fmac_f32 v154, v251, v251\n" \
    "v_mov_b32 v155, v154\n" \
    "s_nop 1\n" \
    "v_permlane32_swap_b32 v154, v155\n" \
    "v_add_f32 v154, v154, v155\n" \
    "v_mov_b32 v155, v154\n" \
    "s_nop 1\n" \
    "v_permlane16_swap_b32 v154, v155\n" \
    "v_add_f32 v154, v154, v155\n" \
    "v_mul_f32 v154, 0x3c2aaaab, v154\n" \
    "v_rsq_f32 v170, v154\n" \
    "s_nop 0\n" \
    "v_mul_f32 v216, v216, v170\n" \
    "v_mul_f32 v217, v217, v170\n" \
    "v_cvt_pk_bf16_f32 v216, v216, v217\n" \
    "v_mul_f32 v218, v218, v170\n" \
    "v_mul_f32 v219, v219, v170\n" \
    "v_cvt_pk_bf16_f32 v217, v218, v219\n" \
    "v_mul_f32 v212, v212, v170\n" \
    "v_mul_f32 v213, v213, v170\n" \
    "v_cvt_pk_bf16_f32 v218, v212, v213\n" \
    "v_mul_f32 v214, v214, v170\n" \
    "v_mul_f32 v215, v215, v170\n" \
    "v_cvt_pk_bf16_f32 v219, v214, v215\n" \
    "v_mul_f32 v236, v236, v170\n" \
    "v_mul_f32 v237, v237, v170\n" \
    "v_cvt_pk_bf16_f32 v236, v236, v237\n" \
    "v_mul_f32 v238, v238, v170\n" \
    "v_mul_f32 v239, v239, v170\n" \
    "v_cvt_pk_bf16_f32 v237, v238, v239\n" \
    "v_mul_f32 v240, v240, v170\n" \
    "v_mul_f32 v241, v241, v170\n" \
    "v_cvt_pk_bf16_f32 v238, v240, v241\n" \
    "v_mul_f32 v242, v242, v170\n" \
    "v_mul_f32 v243, v243, v170\n" \
    "v_cvt_pk_bf16_f32 v239, v242, v243\n" \
    "v_mul_f32 v244, v244, v170\n" \
    "v_mul_f32 v245, v245, v170\n" \
    "v_cvt_pk_bf16_f32 v244, v244, v245\n" \
    "v_mul_f32 v246, v246, v170\n" \
    "v_mul_f32 v247, v247, v170\n" \
    "v_cvt_pk_bf16_f32 v245, v246, v247\n" \
    "v_mul_f32 v248, v248, v170\n" \
    "v_mul_f32 v249, v249, v170\n" \
    "v_cvt_pk_bf16_f32 v246, v248, v249\n" \
    "v_mul_f32 v250, v250, v170\n" \
    "v_mul_f32 v251, v251, v170\n" \
    "v_cvt_pk_bf16_f32 v247, v250, v251\n" \
    "buffer_store_dwordx4 v[216:219], v38, s[48:51], s56 offen sc0 sc1\n" \
    "buffer_store_dwordx4 v[236:239], v38, s[48:51], s56 offen offset:64 sc0 sc1\n" \
    "buffer_store_dwordx4 v[244:247], v38, s[48:51], s56 offen offset:128 sc0 sc1\n" \
    "s_mov_b32 s54, 0\n" \
    "s_branch Lfin%=\n" \
    "Lexit%=:\n" \
    "s_waitcnt vmcnt(0) lgkmcnt(0)\n" \
    "s_nop 7\n" \
    "v_mov_b32 v56, 148740\n" \
    "s_branch Lw4e%=\n" \
    "Lw4%=:\n" \
    "s_sleep 1\n" \
    "Lw4e%=:\n" \
    "ds_read_b32 v57, v56\n" \
    "s_waitcnt lgkmcnt(0)\n" \
    "v_cmp_gt_u32 vcc, 4, v57\n" \
    "s_nop 1\n" \
    "s_cbranch_vccnz Lw4%=\n" \
    "s_lshl_b32 s60, s35, 2\n" \
    "s_add_u32 s60, s60, 148800\n" \
    "s_lshl_b32 s61, s54, 4\n" \
    "s_add_u32 s61, s61, s60\n" \
    "s_sub_u32 s61, s61, 16\n" \
    "v_mov_b32 v56, s60\n" \
    "v_mov_b32 v57, s61\n" \
    "v_mov_b32 v58, 0x80000000\n" \
    "v_mov_b32 v59, 148752\n" \
    "v_mov_b32 v60, s54\n" \
    "ds_write_b32 v56, v58\n" \
    "ds_write_b32 v57, v58\n" \
    "s_mov_b64 exec, 1\n" \
    "ds_add_u32 v59, v60\n" \
    "s_mov_b64 exec, -1\n" \
    "s_branch Lfin%=\n" \
    "Lout%=:\n" \
    "s_setprio 2\n" \
    "s_waitcnt vmcnt(0) lgkmcnt(0)\n" \

#define FAST_CLOB "v1", "v2", "v3", "v4", "v5", "v6", "v7", "v8", "v9", "v10", "v11", "v12", "v13", "v14", "v15", "v16", "v17", "v18", "v19", "v20", "v21", "v22", "v23", "v24", "v25", "v26", "v27", "v28", "v29", "v30", "v31", "v32", "v33", "v34", "v35", "v36", "v37", "v38", "v39", "v40", "v41", "v42", "v43", "v44", "v45", "v46", "v47", "v48", "v49", "v50", "v51", "v52", "v53", "v54", "v55", "v56", "v57", "v58", "v59", "v60", "v61", "v62", "v63", "v64", "v65", "v66", "v67", "v68", "v69", "v70", "v71", "v72", "v73", "v74", "v75", "v76", "v77", "v78", "v79", "v80", "v81", "v82", "v83", "v84", "v85", "v86", "v87", "v88", "v89", "v90", "v91", "v92", "v93", "v94", "v95", "v96", "v97", "v98", "v99", "v100", "v101", "v102", "v103", "v104", "v105", "v106", "v107", "v108", "v109", "v110", "v111", "v112", "v113", "v114", "v115", "v116", "v117", "v118", "v119", "v120", "v121", "v122", "v123", "v124", "v125", "v126", "v127", "v128", "v129", "v130", "v131", "v132", "v133", "v134", "v135", "v136", "v137", "v138", "v139", "v140", "v141", "v142", "v143", "v144", "v145", "v146", "v147", "v148", "v149", "v150", "v151", "v152", "v153", "v154", "v155", "v156", "v157", "v158", "v159", "v160", "v161", "v162", "v163", "v164", "v165", "v166", "v167", "v168", "v169", "v170", "v171", "v172", "v173", "v174", "v175", "v176", "v177", "v178", "v179", "v180", "v181", "v182", "v183", "v184", "v185", "v186", "v187", "v188", "v189", "v190", "v191", "v192", "v193", "v194", "v195", "v196", "v197", "v198", "v199", "v200", "v201", "v202", "v203", "v204", "v205", "v206", "v207", "v208", "v209", "v210", "v211", "v212", "v213", "v214", "v215", "v216", "v217", "v218", "v219", "v220", "v221", "v222", "v223", "v224", "v225", "v226", "v227", "v228", "v229", "v230", "v231", "v232", "v233", "v234", "v235", "v236", "v237", "v238", "v239", "v240", "v241", "v242", "v243", "v244", "v245", "v246", "v247", "v248", "v249", "v250", "v251", "v252", "v253", "v254", "v255", "s34", "s35", "s40", "s41", "s42", "s43", "s44", "s45", "s46", "s47", "s48", "s49", "s50", "s51", "s55", "s56", "s57", "s58", "s59", "s60", "s61", "s62", "s63", "s64", "s65", "s66", "s67", "s68", "s69", "s70", "s71", "s75", "s76", "s77", "s78", "s79", "s80", "s81", "s82", "s83", "s84", "s85", "s86", "s87", "s88", "s89", "s90", "s91", "s92", "s93", "s94", "s95", "s96", "s97", "s98", "s99", "vcc", "scc", "memory"
constexpr int LDS_BYTES = 162304, WO_OFF = 56192, BI_OFF = 74624, HS = 9248, B48 = 36992, BXO = 114688, C_WO = 148744, C_BI = 148740, FSTR = 1040;

DI unsigned pkbf(float a, float b) { f32x2 v = {a, b}; return __builtin_bit_cast(unsigned, __builtin_convertvector(v, bf16x2)); }
DI unsigned pkh(float a, float b) { f32x2 v = {a, b}; return __builtin_bit_cast(unsigned, __builtin_convertvector(v, f16x2)); }
DI unsigned asu(float x) { return __builtin_bit_cast(unsigned, x); }
DI float asf(unsigned x) { return __builtin_bit_cast(float, x); }
#define MFMA_BF(a, b, c) __builtin_amdgcn_mfma_f32_16x16x32_bf16(__builtin_bit_cast(bf16x8, a), __builtin_bit_cast(bf16x8, b), (c), 0, 0, 0)
#define MFMA_H(a, b, c) __builtin_amdgcn_mfma_f32_16x16x32_f16(__builtin_bit_cast(f16x8, a), __builtin_bit_cast(f16x8, b), (c), 0, 0, 0)
#define SBAR() __builtin_amdgcn_sched_barrier(0)
DI void wait_count(unsigned addr, unsigned n) {
    unsigned v;
    asm volatile("Lw%=:\n\ts_sleep 1\n\tds_read_b32 %0, %1\n\ts_waitcnt lgkmcnt(0)\n\tv_cmp_gt_u32 vcc, %2, %0\n\ts_nop 1\n\ts_cbranch_vccnz Lw%="
                 : "=&v"(v) : "v"(addr), "s"(n) : "vcc", "memory");
}
DI float gsum(float x) {
    auto a = __builtin_amdgcn_permlane32_swap(asu(x), asu(x), false, false); x = asf(a[0]) + asf(a[1]);
    auto b = __builtin_amdgcn_permlane16_swap(asu(x), asu(x), false, false); return asf(b[0]) + asf(b[1]);
}
DI float gmax(float x) {
    auto a = __builtin_amdgcn_permlane32_swap(asu(x), asu(x), false, false); x = __builtin_fmaxf(asf(a[0]), asf(a[1]));
    auto b = __builtin_amdgcn_permlane16_swap(asu(x), asu(x), false, false); return __builtin_fmaxf(asf(b[0]), asf(b[1]));
}

DI unsigned tok_off(int wy, int wx, int t, unsigned lane) {
    const unsigned tok = t < 3 ? 16 * t + (lane & 15) : 48, ty = (tok * 37) >> 8, tx = tok - 7 * ty;
    unsigned py = 7 * wy + 3 + ty, px = 7 * wx + 3 + tx;
    py -= py >= IMG ? IMG : 0;
    px -= px >= IMG ? IMG : 0;
    return py * ROWB + px * 192 + (lane >> 4) * 16;
}

DI void slow_window(int idx, const f32x4 cs, const char* x, const unsigned short* mask, char* out) {
    unsigned z = 0;
    asm volatile("" : "+v"(z));
    const unsigned lane = __builtin_amdgcn_mbcnt_hi(~0u, __builtin_amdgcn_mbcnt_lo(~0u, z)), c = lane & 15, g = lane >> 4;
    const int j = idx - 2809, q = (idx * 19785) >> 20;
    const int wy = j < 0 ? q : (j < 54 ? 53 : j - 54), wx = j < 0 ? idx - q * 53 : (j < 54 ? j : 53);
    const __amdgpu_buffer_rsrc_t rx = __builtin_amdgcn_make_buffer_rsrc((void*)x, 0, XBYTES, 0x00020000);
    const __amdgpu_buffer_rsrc_t ro = __builtin_amdgcn_make_buffer_rsrc((void*)out, 0, XBYTES, 0x00020000);
    const unsigned short* mw = mask + (size_t)(wy * NWIN + wx) * 2401;
    u32x4 xf[4][3];
#pragma unroll
    for (int t = 0; t < 4; ++t) {
        const unsigned xo = tok_off(wy, wx, t, lane);
#pragma unroll
        for (int k = 0; k < 3; ++k) xf[t][k] = __builtin_amdgcn_raw_buffer_load_b128(rx, xo + 64 * k, 0, 0);
    }
    unsigned mk[4][7];
#pragma unroll
    for (int t = 0; t < 4; ++t) {
        const unsigned short* row = mw + (t < 3 ? 16 * t + c : 48) * 49;
#pragma unroll
        for (int kt = 0; kt < 3; ++kt) { const u32x2 v = *(const u32x2_a2*)(row + 16 * kt + 4 * g); mk[t][2 * kt] = v[0]; mk[t][2 * kt + 1] = v[1]; }
        mk[t][6] = row[48];
    }
    unsigned op[4][12] = {};
#pragma unroll 1
    for (int hp = 0; hp < 2; ++hp) {
#pragma unroll
        for (int t = 0; t < 4; ++t)
#pragma unroll
            for (int i = 0; i < 6; ++i) op[t][i] = op[t][6 + i];
        const float ce[2] = {hp ? cs[2] : cs[0], hp ? cs[3] : cs[1]};
        const unsigned wb = lane * 16 + hp * 9 * FSTR;
        const unsigned bb = BI_OFF + hp * 2 * HS + (lane ^ (g & 1)) * 16, b48 = BI_OFF + B48 + hp * 512 + c * 16, bx = BXO + hp * 512 + g * 16;
        unsigned kq[2][4][6];
        unsigned vv[3][8];
#pragma unroll
        for (int kind = 0; kind < 3; ++kind) {
            f32x4 acc[4][3] = {};
#pragma unroll
            for (int m = 0; m < 3; ++m)
#pragma unroll
                for (int k = 0; k < 3; ++k) {
                    const u32x4 w = *LDSP(u32x4, wb + ((kind * 6 + m) * 3 + k) * FSTR);
#pragma unroll
                    for (int t = 0; t < 4; ++t) acc[t][m] = kind < 2 ? MFMA_BF(w, xf[t][k], acc[t][m]) : MFMA_BF(xf[t][k], w, acc[t][m]);
                }
#pragma unroll
            for (int t = 0; t < 4; ++t) {
                if (kind < 2) {
#pragma unroll
                    for (int e = 0; e < 2; ++e) {
                        float v[6], ss = 0.f;
#pragma unroll
                        for (int i = 0; i < 6; ++i) { v[i] = acc[t][(6 * e + i) >> 2][(6 * e + i) & 3]; ss = __builtin_fmaf(v[i], v[i], ss); }
                        const float f = __builtin_amdgcn_rsqf(gsum(ss) + 1e-12f) * (kind ? ce[e] : 1.f);

#pragma unroll
                        for (int i = 0; i < 3; ++i) kq[kind][t][3 * e + i] = pkh(v[2 * i] * f, v[2 * i + 1] * f);
                    }
                } else {
#pragma unroll
                    for (int m = 0; m < 3; ++m) { vv[m][2 * t] = pkbf(acc[t][m][0], acc[t][m][1]); vv[m][2 * t + 1] = pkbf(acc[t][m][2], acc[t][m][3]); }
                }
            }
            SBAR();
        }
        wait_count(C_BI, 4);
#pragma unroll
        for (int t = 0; t < 4; ++t) {
#pragma unroll
            for (int i = 0; i < 7; ++i) asm volatile("" : "+v"(mk[t][i]));
#pragma unroll
            for (int e = 0; e < 2; ++e) {
                float p[13];
                const unsigned* qq = kq[1][t];
                const u32x4 qf = e ? u32x4{0u, qq[3], qq[4], qq[5]} : u32x4{qq[0], qq[1], qq[2], 0u};
#pragma unroll
                for (int kt = 0; kt < 4; ++kt) {
                    const unsigned ba = t == 3 ? bx + e * 256 + kt * 64 : (kt < 3 ? bb + e * HS + (t * 3 + kt) * 1024 : (b48 ^ (e * 16)) + e * 256 + t * 1024);
                    const unsigned* kk = kq[0][kt] + 2 * e;
                    const f32x4 sc = MFMA_H((u32x4{kk[0], kk[1], kk[2], kk[3]}), qf, *LDSP(f32x4, ba));
#pragma unroll
                    for (int r = 0; r < (kt < 3 ? 4 : 1); ++r) {
                        const unsigned w = mk[t][2 * kt + (r >> 1)];
                        p[4 * kt + r] = __builtin_fmaf(asf((r & 1) ? w & 0xffff0000u : w << 16), LOG2E, sc[r]);
                    }
                }
                p[12] = g == 0 ? p[12] : -__builtin_inff();
                float m = p[12];
#pragma unroll
                for (int i = 0; i < 12; ++i) m = __builtin_fmaxf(m, p[i]);
                m = gmax(m);
                float l = 0.f;
#pragma unroll
                for (int i = 0; i < 13; ++i) { p[i] = __builtin_amdgcn_exp2f(p[i] - m); l += p[i]; }
                const float rl = __builtin_amdgcn_rcpf(gsum(l));
                const u32x4 p0 = {pkbf(p[0], p[1]), pkbf(p[2], p[3]), pkbf(p[4], p[5]), pkbf(p[6], p[7])};
                const u32x4 p1 = {pkbf(p[8], p[9]), pkbf(p[10], p[11]), pkbf(p[12], 0.f), 0u};
                f32x4 o[2];
#pragma unroll
                for (int m2 = 0; m2 < 2; ++m2) {
                    const unsigned* v = vv[e + m2];
                    o[m2] = MFMA_BF((u32x4{v[0], v[1], v[2], v[3]}), p0, f32x4{});
                    o[m2] = MFMA_BF((u32x4{v[4], v[5], v[6], v[7]}), p1, o[m2]);
                }
#pragma unroll
                for (int i = 0; i < 3; ++i) {
                    const int s = 2 * e + 2 * i;
                    op[t][6 + 3 * e + i] = pkbf(o[s >> 2][s & 3] * rl, o[s >> 2][(s & 3) + 1] * rl);
                }
            }
            SBAR();
        }
    }
    wait_count(C_WO, 1);
    unsigned ln2 = lane;
    asm volatile("" : "+v"(ln2));
    f32x4 y[4][6] = {};
#pragma unroll
    for (int m = 0; m < 6; ++m)
#pragma unroll
        for (int k = 0; k < 3; ++k) {
            const u32x4 w = *LDSP(u32x4, WO_OFF + lane * 16 + (m * 3 + k) * 1024);
#pragma unroll
            for (int t = 0; t < 4; ++t) y[t][m] = MFMA_BF(w, (u32x4{op[t][4 * k], op[t][4 * k + 1], op[t][4 * k + 2], op[t][4 * k + 3]}), y[t][m]);
        }
#pragma unroll
    for (int t = 0; t < 4; ++t) {
        float ss = 0.f;
#pragma unroll
        for (int i = 0; i < 24; ++i) ss = __builtin_fmaf(y[t][i >> 2][i & 3], y[t][i >> 2][i & 3], ss);
        const float rs = __builtin_amdgcn_rsqf(__builtin_fmaf(gsum(ss), 1.f / 96.f, 1e-5f * 96.f * 96.f));
        const unsigned so = (t < 3 || (ln2 & 15) == 0) ? tok_off(wy, wx, t, ln2) : 0x80000000u;
#pragma unroll
        for (int p = 0; p < 3; ++p) {
            const f32x4 a = y[t][2 * p], b = y[t][2 * p + 1];
            __builtin_amdgcn_raw_buffer_store_b128(u32x4{pkbf(a[0] * rs, a[1] * rs), pkbf(a[2] * rs, a[3] * rs), pkbf(b[0] * rs, b[1] * rs), pkbf(b[2] * rs, b[3] * rs)},
                                                   ro, so + 64 * p, 0, 0);
        }
    }
}

__global__ __launch_bounds__(512, 2) void swin_attn_kernel(
    const char* __restrict__ x, const char* __restrict__ wqkv, const char* __restrict__ wo,
    const unsigned* __restrict__ lscale, const unsigned short* __restrict__ w1,
    const unsigned short* __restrict__ w2, const unsigned* __restrict__ relc,
    const int* __restrict__ relidx, const unsigned short* __restrict__ mask, char* __restrict__ out
    ) {
#if defined(__HIP_DEVICE_COMPILE__)
    __shared__ __attribute__((aligned(128))) char smem[LDS_BYTES];
    unsigned tid = __builtin_amdgcn_workitem_id_x();
    int idx, stride, rem;
    f32x4 cs;
    asm volatile(FAST_ASM
                 : "+{v0}"(tid), "={s52}"(idx), "={s53}"(stride), "={s54}"(rem), "={s[36:39]}"(cs)
                 : "{s[72:73]}"(__builtin_amdgcn_kernarg_segment_ptr()), "{s74}"(__builtin_amdgcn_workgroup_id_x()),
                   "s"((unsigned)(uintptr_t)(LDS_AS char*)smem)
                 : FAST_CLOB);
    for (; rem > 0; --rem) {
        slow_window(idx, cs, x, mask, out);
        idx += stride;
    }
#endif
}

extern "C" void kernel_launch(void* const* d_in, const int* in_sizes, int n_in,
                              void* d_out, int out_size, void* d_ws, size_t ws_size,
                              hipStream_t stream) {
    swin_attn_kernel<<<256, 512, 0, stream>>>(
        (const char*)d_in[0], (const char*)d_in[1], (const char*)d_in[2],
        (const unsigned*)d_in[3], (const unsigned short*)d_in[4], (const unsigned short*)d_in[5],
        (const unsigned*)d_in[6], (const int*)d_in[7], (const unsigned short*)d_in[8], (char*)d_out
        );
}
```
